```python
import torch

def _softmax(x, axis=-1):
    return torch.softmax(x, dim=axis)
def _gelu(x):
    return torch.nn.functional.gelu(x, approximate="tanh")
def _silu(x):
    return torch.nn.functional.silu(x)
def _sigmoid(x):
    return torch.sigmoid(x)
def _layernorm(x, axis=-1, eps=1e-5):
    m = x.mean(dim=axis, keepdim=True)
    v = x.var(dim=axis, keepdim=True, unbiased=False)
    return (x - m) / torch.sqrt(v + eps)
def _scatter_add(tgt, idx, src, axis=0):
    return tgt.clone().index_add_(axis, idx, src)
def _segment_sum(src, seg, n):
    out = torch.zeros((n,) + src.shape[1:], dtype=src.dtype)
    return out.index_add_(0, seg, src)
def _pad(x, pw):
    flat = tuple(v for pair in reversed(pw) for v in pair)
    return torch.nn.functional.pad(x, flat)

kernel_name = 'interleave_layers__mha_block_cached__glu_gated_bilinear__decode'

def setup_inputs():

    _w = (lambda a: (lambda t: t.to(torch.bfloat16) if t.is_floating_point() else t)(torch.as_tensor(a)))
    def _A_setup():
        B, Sq, D = 2, 1, 448
        H, Sk, Dh = 16, 8192, 256
        return {
            "x": _w((lambda *s: torch.randn(*s, dtype=torch.bfloat16) * 0.05)(B, Sq, D)),
            "k_cache": _w((lambda *s: torch.randn(*s, dtype=torch.bfloat16) * 0.8)(B, H, Sk, Dh)),
            "v_cache": _w((lambda *s: torch.randn(*s, dtype=torch.bfloat16) * 1.6)(B, H, Sk, Dh)),
            "wq": _w((lambda *s: torch.randn(*s, dtype=torch.bfloat16) * 0.8)(D, H * Dh) * (D ** -0.5)),
            "wo": _w((lambda *s: torch.randn(*s, dtype=torch.bfloat16) * 1.6)(H * Dh, D) * ((H * Dh) ** -0.5)),
        }
    _A_in = _A_setup()

    def _B_setup():
        B, S, D, F = 2, 1, 448, 1792
        return {
            "x": _w((lambda *s: torch.randn(*s, dtype=torch.bfloat16) * 0.05)(B, S, D)),
            "w_gate": _w((lambda *s: torch.randn(*s, dtype=torch.bfloat16) * 0.4)(D, F) * (D ** -0.5)),
            "w_up": _w((lambda *s: torch.randn(*s, dtype=torch.bfloat16) * 0.4)(D, F) * (D ** -0.5)),
            "w_down": _w((lambda *s: torch.randn(*s, dtype=torch.bfloat16) * 0.4)(F, D) * (F ** -0.5)),
        }
    _B_in = _B_setup()

    return {
        "A_x": _A_in["x"],
        "A_k_cache": _A_in["k_cache"],
        "A_v_cache": _A_in["v_cache"],
        "A_wq": _A_in["wq"],
        "A_wo": _A_in["wo"],
        "B_x": _B_in["x"],
        "B_w_gate": _B_in["w_gate"],
        "B_w_up": _B_in["w_up"],
        "B_w_down": _B_in["w_down"],
    }

def reference(A_x, A_k_cache, A_v_cache, A_wq, A_wo, B_x, B_w_gate, B_w_up, B_w_down):
    def _A_ref(x, k_cache, v_cache, wq, wo):
        B, Sq, D = x.shape
        H, Dh = 16, 256
        q = (lambda x, *d: x.permute(*d))((x @ wq).reshape(B, Sq, H, Dh), 0, 2, 1, 3)
        scores = torch.einsum("bhqd,bhkd->bhqk", q, k_cache) * (Dh ** -0.5)
        p = _softmax(scores, axis=-1)
        o = torch.einsum("bhqk,bhkd->bhqd", p, v_cache)
        return (lambda x, *d: x.permute(*d))(o, 0, 2, 1, 3).reshape(B, Sq, H * Dh) @ wo

    def _B_ref(x, w_gate, w_up, w_down):
        g = torch.einsum("bsd,df->bsf", x, w_gate)
        u = torch.einsum("bsd,df->bsf", x, w_up)
        h = g * u
        return torch.einsum("bsf,fd->bsd", h, w_down)

    x = A_x
    x = x + _A_ref(_layernorm(x, axis=-1), A_k_cache, A_v_cache, A_wq, A_wo)
    x = x + _B_ref(_layernorm(x, axis=-1), B_w_gate, B_w_up, B_w_down)
    x = x + _A_ref(_layernorm(x, axis=-1), A_k_cache, A_v_cache, A_wq, A_wo)
    x = x + _B_ref(_layernorm(x, axis=-1), B_w_gate, B_w_up, B_w_down)
    return x
```

```cpp
#include <hip/hip_runtime.h>
#include <hip/hip_bf16.h>
#include <stdint.h>

constexpr int NB = 2, DM = 448, NH = 16, SK = 8192, DH = 256, FF = 1792, HD = NH * DH;
constexpr float LN_EPS = 1e-5f;
constexpr float QSCALE = 0.0625f * 1.4426950408889634f;

typedef unsigned u32x4 __attribute__((ext_vector_type(4)));
typedef float f32x4 __attribute__((ext_vector_type(4)));
typedef short bf16x8 __attribute__((ext_vector_type(8)));
typedef unsigned short u16;

__device__ __forceinline__ float bf2f(u16 b) { return __builtin_bit_cast(float, (unsigned)b << 16); }
__device__ __forceinline__ float bflo(unsigned w) { return __builtin_bit_cast(float, w << 16); }
__device__ __forceinline__ float bfhi(unsigned w) { return __builtin_bit_cast(float, w & 0xffff0000u); }
__device__ __forceinline__ unsigned cvtpk(float lo, float hi) {
  unsigned r; asm("v_cvt_pk_bf16_f32 %0, %1, %2" : "=v"(r) : "v"(lo), "v"(hi)); return r;
}
__device__ __forceinline__ f32x4 mfma(u32x4 a, u32x4 b, f32x4 c) {
  return __builtin_amdgcn_mfma_f32_16x16x32_bf16(__builtin_bit_cast(bf16x8, a), __builtin_bit_cast(bf16x8, b), c, 0, 0, 0);
}
template <int CTRL> __device__ __forceinline__ float dpp(float x) {
  return __builtin_bit_cast(float, __builtin_amdgcn_mov_dpp(__builtin_bit_cast(int, x), CTRL, 0xf, 0xf, true));
}
constexpr int XOR1 = 0xB1, XOR2 = 0x4E;
__device__ __forceinline__ float xrow16_max(float x) {
  auto s = __builtin_amdgcn_permlane16_swap(__float_as_uint(x), __float_as_uint(x), false, false);
  x = fmaxf(__uint_as_float(s[0]), __uint_as_float(s[1]));
  auto t = __builtin_amdgcn_permlane32_swap(__float_as_uint(x), __float_as_uint(x), false, false);
  return fmaxf(__uint_as_float(t[0]), __uint_as_float(t[1]));
}
__device__ __forceinline__ float xrow16_sum(float x) {
  auto s = __builtin_amdgcn_permlane16_swap(__float_as_uint(x), __float_as_uint(x), false, false);
  x = __uint_as_float(s[0]) + __uint_as_float(s[1]);
  auto t = __builtin_amdgcn_permlane32_swap(__float_as_uint(x), __float_as_uint(x), false, false);
  return __uint_as_float(t[0]) + __uint_as_float(t[1]);
}
__device__ __forceinline__ float wave_sum(float v) {
#pragma unroll
  for (int o = 32; o > 0; o >>= 1) v += __shfl_xor(v, o);
  return v;
}

constexpr size_t WS_X = 0;
constexpr size_t WS_Q = 4096;
constexpr size_t WS_PO = WS_Q + NB * HD;
constexpr size_t WS_PML = WS_PO + 32 * 32 * 256;
constexpr size_t WS_PA = WS_PML + 32 * 32 * 2;
constexpr size_t WS_H = WS_PA + 16 * NB * DM;
constexpr size_t WS_PD = WS_H + NB * FF;

__device__ __forceinline__ void block_ln(float* xs, float* red) {
  const int tid = threadIdx.x, lane = tid & 63, w = tid >> 6;
  if (w < 2) {
    float s = 0.f;
    for (int i = lane; i < DM; i += 64) s += xs[w * DM + i];
    s = wave_sum(s);
    const float mean = s * (1.f / DM);
    float v = 0.f;
    for (int i = lane; i < DM; i += 64) { float d = xs[w * DM + i] - mean; v += d * d; }
    v = wave_sum(v);
    const float rs = 1.f / sqrtf(v * (1.f / DM) + LN_EPS);
    for (int i = lane; i < DM; i += 64) xs[w * DM + i] = (xs[w * DM + i] - mean) * rs;
  }
  __syncthreads();
}

template <int KR, int N>
__device__ __forceinline__ void gemv64(const u16* __restrict__ W, int k0, int c0, const float* in, int istr, float* red, float& out0) {
  const int tid = threadIdx.x, cg = tid & 7, ks = tid >> 3;
  float a0[8], a1[8];
#pragma unroll
  for (int j = 0; j < 8; ++j) { a0[j] = 0.f; a1[j] = 0.f; }
  for (int k = ks; k < KR; k += 32) {
    const u32x4 w = *reinterpret_cast<const u32x4*>(W + (size_t)(k0 + k) * N + c0 + cg * 8);
    const float i0 = in[k], i1 = in[istr + k];
#pragma unroll
    for (int j = 0; j < 4; ++j) {
      a0[2 * j] = fmaf(i0, bflo(w[j]), a0[2 * j]); a0[2 * j + 1] = fmaf(i0, bfhi(w[j]), a0[2 * j + 1]);
      a1[2 * j] = fmaf(i1, bflo(w[j]), a1[2 * j]); a1[2 * j + 1] = fmaf(i1, bfhi(w[j]), a1[2 * j + 1]);
    }
  }
#pragma unroll
  for (int j = 0; j < 8; ++j) { red[ks * 128 + cg * 8 + j] = a0[j]; red[ks * 128 + 64 + cg * 8 + j] = a1[j]; }
  __syncthreads();
  float s = 0.f;
  if (tid < 128) { for (int i = 0; i < 32; ++i) s += red[i * 128 + tid]; }
  out0 = s;
  __syncthreads();
}

__global__ void __launch_bounds__(256) k_qproj(const u16* __restrict__ xin, const u16* __restrict__ wq, float* ws, int layer) {
  __shared__ float xs[NB * DM];
  __shared__ float red[32 * 128];
  const int tid = threadIdx.x;
  for (int i = tid; i < NB * DM; i += 256) {
    float v;
    if (layer == 0) v = bf2f(xin[i]);
    else { v = ws[WS_X + 1024 + i]; for (int s = 0; s < 7; ++s) v += ws[WS_PD + s * NB * DM + i]; }
    xs[i] = v;
    if (blockIdx.x == 0) ws[WS_X + i] = v;
  }
  __syncthreads();
  block_ln(xs, red);
  float o;
  gemv64<DM, HD>(wq, 0, blockIdx.x * 64, xs, DM, red, o);
  if (tid < 128) ws[WS_Q + (tid >> 6) * HD + blockIdx.x * 64 + (tid & 63)] = o * QSCALE;
}

__global__ void __launch_bounds__(256) k_attn(const u16* __restrict__ kc, const u16* __restrict__ vc, float* ws) {
  const int tid = threadIdx.x, lane = tid & 63, w = tid >> 6;
  const int bh = blockIdx.x >> 3, split = blockIdx.x & 7;
  const int n = lane & 15, g = lane >> 4;
  const float* q = ws + WS_Q + (size_t)(bh >> 4) * HD + (bh & 15) * DH;
  u32x4 qf[4];
#pragma unroll
  for (int s = 0; s < 4; ++s) {
    const int d0 = 64 * s + 32 * (n & 1) + 8 * g;
#pragma unroll
    for (int j = 0; j < 4; ++j) {
      const float a = q[d0 + 2 * j], b = q[d0 + 2 * j + 1];
      const unsigned hi = cvtpk(a, b);
      const unsigned lo = cvtpk(a - bflo(hi), b - bfhi(hi));
      qf[s][j] = (n & 2) ? lo : hi;
    }
  }
  unsigned pm[4];
#pragma unroll
  for (int j = 0; j < 4; ++j) pm[j] = (((n & 7) >> 1) == j) ? ((n & 1) ? 0xffff0000u : 0x0000ffffu) : 0u;
  const int key0 = split * 1024 + w * 256;
  const u16* kb = kc + ((size_t)bh * SK + key0) * DH;
  const u16* vb = vc + ((size_t)bh * SK + key0) * DH;
  const int koff = (n >> 1) * DH + 32 * (n & 1) + 8 * g;
  const int voff = (2 * g) * DH + 8 * n;
  float m = -INFINITY, l = 0.f;
  f32x4 acc0 = {0, 0, 0, 0}, acc1 = {0, 0, 0, 0};
  for (int c = 0; c < 8; ++c) {
    u32x4 kf[16], vf[16];
#pragma unroll
    for (int gi = 0; gi < 4; ++gi)
#pragma unroll
      for (int s = 0; s < 4; ++s)
        kf[gi * 4 + s] = __builtin_nontemporal_load(reinterpret_cast<const u32x4*>(kb + (size_t)(c * 32 + gi * 8) * DH + koff + 64 * s));
#pragma unroll
    for (int gi = 0; gi < 4; ++gi)
#pragma unroll
      for (int r = 0; r < 2; ++r)
#pragma unroll
        for (int hf = 0; hf < 2; ++hf)
          vf[gi * 4 + r * 2 + hf] = __builtin_nontemporal_load(reinterpret_cast<const u32x4*>(vb + (size_t)(c * 32 + gi * 8 + r) * DH + voff + 128 * hf));
    float sc[8];
    float cm = -INFINITY;
#pragma unroll
    for (int gi = 0; gi < 4; ++gi) {
      f32x4 d = {0, 0, 0, 0};
#pragma unroll
      for (int s = 0; s < 4; ++s) d = mfma(kf[gi * 4 + s], qf[s], d);
      float t0 = (n & 1) ? d[1] : d[0], t1 = (n & 1) ? d[3] : d[2];
      t0 += dpp<XOR1>(t0); t1 += dpp<XOR1>(t1);
      t0 += dpp<XOR2>(t0); t1 += dpp<XOR2>(t1);
      sc[gi * 2] = t0; sc[gi * 2 + 1] = t1;
      cm = fmaxf(cm, fmaxf(t0, t1));
    }
    cm = xrow16_max(cm);
    const float mn = fmaxf(m, cm);
    const float alpha = __builtin_amdgcn_exp2f(m - mn);
    m = mn; l *= alpha; acc0 *= alpha; acc1 *= alpha;
#pragma unroll
    for (int gi = 0; gi < 4; ++gi)
#pragma unroll
      for (int r = 0; r < 2; ++r) {
        const float p = __builtin_amdgcn_exp2f(sc[gi * 2 + r] - m);
        l += p;
        const unsigned hi = cvtpk(p, p);
        const float rl = p - bfhi(hi);
        const unsigned lo = cvtpk(rl, rl);
        const unsigned sel = (n & 8) ? lo : hi;
        u32x4 pb = {sel & pm[0], sel & pm[1], sel & pm[2], sel & pm[3]};
        acc0 = mfma(vf[gi * 4 + r * 2 + 0], pb, acc0);
        acc1 = mfma(vf[gi * 4 + r * 2 + 1], pb, acc1);
      }
  }
  l = xrow16_sum(l);
  const int slot = bh * 32 + split * 4 + w;
  float* po = ws + WS_PO + (size_t)slot * 256;
#pragma unroll
  for (int r = 0; r < 4; ++r) {
    float a0 = acc0[r], a1 = acc1[r];
    a0 += dpp<0x128>(a0); a1 += dpp<0x128>(a1);
    if (n < 8) { po[8 * (4 * g + r) + n] = a0; po[128 + 8 * (4 * g + r) + n] = a1; }
  }
  if (lane == 0) { ws[WS_PML + slot * 2] = m; ws[WS_PML + slot * 2 + 1] = l; }
}

__global__ void __launch_bounds__(256) k_wo(const u16* __restrict__ wo, float* ws) {
  __shared__ float os[NB * 256];
  __shared__ float red[32 * 128];
  const int tid = threadIdx.x, h = blockIdx.y;
  for (int b = 0; b < NB; ++b) {
    const int bh = b * 16 + h;
    float M = -INFINITY;
    for (int s = 0; s < 32; ++s) M = fmaxf(M, ws[WS_PML + (bh * 32 + s) * 2]);
    float L = 0.f, o = 0.f;
    for (int s = 0; s < 32; ++s) {
      const float wgt = __builtin_amdgcn_exp2f(ws[WS_PML + (bh * 32 + s) * 2] - M);
      L += wgt * ws[WS_PML + (bh * 32 + s) * 2 + 1];
      o += wgt * ws[WS_PO + (size_t)(bh * 32 + s) * 256 + tid];
    }
    os[b * 256 + tid] = o / L;
  }
  __syncthreads();
  float r;
  gemv64<256, DM>(wo, h * 256, blockIdx.x * 64, os, 256, red, r);
  if (tid < 128) ws[WS_PA + (size_t)h * NB * DM + (tid >> 6) * DM + blockIdx.x * 64 + (tid & 63)] = r;
}

__global__ void __launch_bounds__(256) k_gateup(const u16* __restrict__ wg, const u16* __restrict__ wu, float* ws) {
  __shared__ float xs[NB * DM];
  __shared__ float red[32 * 128];
  const int tid = threadIdx.x;
  for (int i = tid; i < NB * DM; i += 256) {
    float v = ws[WS_X + i];
    for (int s = 0; s < 16; ++s) v += ws[WS_PA + s * NB * DM + i];
    xs[i] = v;
    if (blockIdx.x == 0) ws[WS_X + 1024 + i] = v;
  }
  __syncthreads();
  block_ln(xs, red);
  float g, u;
  gemv64<DM, FF>(wg, 0, blockIdx.x * 64, xs, DM, red, g);
  gemv64<DM, FF>(wu, 0, blockIdx.x * 64, xs, DM, red, u);
  if (tid < 128) ws[WS_H + (tid >> 6) * FF + blockIdx.x * 64 + (tid & 63)] = g * u;
}

__global__ void __launch_bounds__(256) k_down(const u16* __restrict__ wd, float* ws) {
  __shared__ float hs[NB * 256];
  __shared__ float red[32 * 128];
  const int tid = threadIdx.x, ks = blockIdx.y;
  hs[tid] = ws[WS_H + ks * 256 + tid];
  hs[256 + tid] = ws[WS_H + FF + ks * 256 + tid];
  __syncthreads();
  float r;
  gemv64<256, DM>(wd, ks * 256, blockIdx.x * 64, hs, 256, red, r);
  if (tid < 128) ws[WS_PD + (size_t)ks * NB * DM + (tid >> 6) * DM + blockIdx.x * 64 + (tid & 63)] = r;
}

__global__ void __launch_bounds__(256) k_final(float* ws, __hip_bfloat16* out) {
  for (int i = threadIdx.x; i < NB * DM; i += 256) {
    float v = ws[WS_X + 1024 + i];
    for (int s = 0; s < 7; ++s) v += ws[WS_PD + s * NB * DM + i];
    out[i] = __float2bfloat16(v);
  }
}

extern "C" void kernel_launch(void* const* d_in, const int* in_sizes, int n_in, void* d_out, int out_size, void* d_ws, size_t ws_size,
                              hipStream_t stream) {
  const u16* x = (const u16*)d_in[0];
  const u16* kc = (const u16*)d_in[1];
  const u16* vc = (const u16*)d_in[2];
  const u16* wq = (const u16*)d_in[3];
  const u16* wo = (const u16*)d_in[4];
  const u16* wg = (const u16*)d_in[6];
  const u16* wu = (const u16*)d_in[7];
  const u16* wd = (const u16*)d_in[8];
  float* ws = (float*)d_ws;
  for (int layer = 0; layer < 2; ++layer) {
    k_qproj<<<64, 256, 0, stream>>>(x, wq, ws, layer);
    k_attn<<<256, 256, 0, stream>>>(kc, vc, ws);
    k_wo<<<dim3(7, 16), 256, 0, stream>>>(wo, ws);
    k_gateup<<<28, 256, 0, stream>>>(wg, wu, ws);
    k_down<<<dim3(7, 7), 256, 0, stream>>>(wd, ws);
  }
  k_final<<<1, 256, 0, stream>>>(ws, (__hip_bfloat16*)d_out);
}
```

```cpp
#define X19E
#define KSTR
#include <hip/hip_runtime.h>
#include <stdint.h>

constexpr int DM = 448, NH = 16, SK = 8192, DH = 256, FF = 1792, HD = NH * DH;
constexpr int NW_CU = 16, NG_CU = 14, ATT0 = 32, NBLK = 256;
constexpr unsigned XQ = 0;
constexpr unsigned X1 = 65536;
constexpr unsigned X2 = X1 + 4 * 16 * 1792;
constexpr unsigned XP = X2 + 4 * 14 * 1792;
constexpr unsigned XEND = XP + 4 * 16 * 4 * 14 * 272;
constexpr unsigned XC = XEND + 256;
constexpr unsigned XEND2 = XC + 16 * 14 * 4096;
constexpr unsigned XDBG = (XEND2 + 4095) / 4096 * 4096;
static_assert(XEND == 1255424, "generated code assumes this layout");
static_assert(XEND2 == 2173184, "generated code assumes this layout");
typedef unsigned short u16;

#define ATT_ASM \
  "s_nop 1\n" \
  "s_load_dwordx2 s[40:41], %[ka], 8\n" \
  "s_load_dwordx2 s[44:45], %[ka], 16\n" \
  "s_load_dwordx2 s[48:49], %[ka], 64\n" \
  "s_load_dwordx2 s[76:77], %[ka], 0\n" \
  "s_load_dwordx2 s[84:85], %[ka], 24\n" \
  "s_mov_b32 s52, %[kvoff]\n" \
  "s_mov_b32 s59, %[w]\n" \
  "s_lshl_b32 s69, s59, 15\n" \
  "v_mbcnt_lo_u32_b32 v62, -1, 0\n" \
  "v_mbcnt_hi_u32_b32 v62, -1, v62\n" \
  "v_lshlrev_b32 v0, 2, v62\n" \
  "v_and_b32 v60, 15, v62\n" \
  "v_lshrrev_b32 v61, 4, v62\n" \
  "s_movk_i32 s61, 0x2400\n" \
  "v_lshrrev_b32 v8, 1, v60\n" \
  "v_mul_u32_u24 v8, s61, v8\n" \
  "v_and_b32 v9, 1, v60\n" \
  "v_lshlrev_b32 v10, 6, v9\n" \
  "v_lshlrev_b32 v11, 4, v61\n" \
  "v_add3_u32 v1, v8, v10, v11\n" \
  "v_mul_u32_u24 v8, s61, v61\n" \
  "v_lshl_add_u32 v2, v60, 4, v8\n" \
  "v_lshlrev_b32 v3, 4, v62\n" \
  "v_add_u32 v3, s69, v3\n" \
  "s_movk_i32 s61, 0x380\n" \
  "v_mul_u32_u24 v42, s61, v9\n" \
  "v_lshl_add_u32 v42, v61, 1, v42\n" \
  "v_lshlrev_b32 v43, 13, v61\n" \
  "v_lshl_add_u32 v43, v60, 4, v43\n" \
  "s_mov_b32 s54, %[xoff]\n" \
  "s_mov_b32 s56, %[has19]\n" \
  "s_mov_b32 s82, %[sq]\n" \
  "s_mov_b32 s58, %[sp]\n" \
  "s_waitcnt lgkmcnt(0)\n" \
  "s_and_b32 s41, s41, 0xffff\n" \
  "s_mov_b32 s42, 0x08000000\n" \
  "s_mov_b32 s43, 0x00020000\n" \
  "s_and_b32 s45, s45, 0xffff\n" \
  "s_mov_b32 s46, 0x08000000\n" \
  "s_mov_b32 s47, 0x00020000\n" \
  "s_and_b32 s49, s49, 0xffff\n" \
  "s_mov_b32 s50, %[xend]\n" \
  "s_mov_b32 s51, 0x00020000\n" \
  "s_and_b32 s77, s77, 0xffff\n" \
  "s_mov_b32 s78, 1792\n" \
  "s_mov_b32 s79, 0x00020000\n" \
  "s_and_b32 s85, s85, 0xffff\n" \
  "s_mov_b32 s86, 3670016\n" \
  "s_mov_b32 s87, 0x00020000\n" \
  "s_mov_b32 s61, 1255552\n" \
  "buffer_load_dword v59, off, s[48:51], s61 sc1\n" \
  "buffer_load_ushort v40, v42, s[76:79], %[cqx] offen offset:0\n" \
  "buffer_load_ushort v41, v42, s[76:79], %[cqx] offen offset:8\n" \
  "s_add_u32 s60, %[cqw], 32768\n" \
  "buffer_load_dwordx4 v[8:11], v43, s[84:87], %[cqw] offen offset:0\n" \
  "buffer_load_dwordx4 v[12:15], v43, s[84:87], %[cqw] offen offset:256\n" \
  "buffer_load_dwordx4 v[16:19], v43, s[84:87], s60 offen offset:0\n" \
  "buffer_load_dwordx4 v[20:23], v43, s[84:87], s60 offen offset:256\n" \
  "s_add_u32 s60, s52, 0\n" \
  "s_add_u32 m0, s69, 0\n" \
  "s_nop 0\n" \
  "buffer_load_dwordx4 v1, s[40:43], s60 offen offset:0 nt lds\n" \
  "s_add_u32 s60, s52, 0\n" \
  "s_add_u32 m0, s69, 896\n" \
  "s_nop 0\n" \
  "buffer_load_dwordx4 v1, s[40:43], s60 offen offset:128 nt lds\n" \
  "s_add_u32 s60, s52, 0\n" \
  "s_add_u32 m0, s69, 1792\n" \
  "s_nop 0\n" \
  "buffer_load_dwordx4 v1, s[40:43], s60 offen offset:256 nt lds\n" \
  "s_add_u32 s60, s52, 0\n" \
  "s_add_u32 m0, s69, 2688\n" \
  "s_nop 0\n" \
  "buffer_load_dwordx4 v1, s[40:43], s60 offen offset:384 nt lds\n" \
  "s_add_u32 s60, s52, 0\n" \
  "s_add_u32 m0, s69, 4096\n" \
  "s_nop 0\n" \
  "buffer_load_dwordx4 v2, s[44:47], s60 offen offset:0 nt lds\n" \
  "s_add_u32 s60, s52, 36864\n" \
  "s_add_u32 m0, s69, 5120\n" \
  "s_nop 0\n" \
  "buffer_load_dwordx4 v2, s[44:47], s60 offen offset:0 nt lds\n" \
  "s_add_u32 s60, s52, 0\n" \
  "s_add_u32 m0, s69, 5888\n" \
  "s_nop 0\n" \
  "buffer_load_dwordx4 v2, s[44:47], s60 offen offset:256 nt lds\n" \
  "s_add_u32 s60, s52, 36864\n" \
  "s_add_u32 m0, s69, 6912\n" \
  "s_nop 0\n" \
  "buffer_load_dwordx4 v2, s[44:47], s60 offen offset:256 nt lds\n" \
  "s_add_u32 s60, s52, 512\n" \
  "s_add_u32 m0, s69, 8192\n" \
  "s_nop 0\n" \
  "buffer_load_dwordx4 v1, s[40:43], s60 offen offset:0 nt lds\n" \
  "s_add_u32 s60, s52, 512\n" \
  "s_add_u32 m0, s69, 9088\n" \
  "s_nop 0\n" \
  "buffer_load_dwordx4 v1, s[40:43], s60 offen offset:128 nt lds\n" \
  "s_add_u32 s60, s52, 512\n" \
  "s_add_u32 m0, s69, 9984\n" \
  "s_nop 0\n" \
  "buffer_load_dwordx4 v1, s[40:43], s60 offen offset:256 nt lds\n" \
  "s_add_u32 s60, s52, 512\n" \
  "s_add_u32 m0, s69, 10880\n" \
  "s_nop 0\n" \
  "buffer_load_dwordx4 v1, s[40:43], s60 offen offset:384 nt lds\n" \
  "s_add_u32 s60, s52, 512\n" \
  "s_add_u32 m0, s69, 12288\n" \
  "s_nop 0\n" \
  "buffer_load_dwordx4 v2, s[44:47], s60 offen offset:0 nt lds\n" \
  "s_add_u32 s60, s52, 37376\n" \
  "s_add_u32 m0, s69, 13312\n" \
  "s_nop 0\n" \
  "buffer_load_dwordx4 v2, s[44:47], s60 offen offset:0 nt lds\n" \
  "s_add_u32 s60, s52, 512\n" \
  "s_add_u32 m0, s69, 14080\n" \
  "s_nop 0\n" \
  "buffer_load_dwordx4 v2, s[44:47], s60 offen offset:256 nt lds\n" \
  "s_add_u32 s60, s52, 37376\n" \
  "s_add_u32 m0, s69, 15104\n" \
  "s_nop 0\n" \
  "buffer_load_dwordx4 v2, s[44:47], s60 offen offset:256 nt lds\n" \
  "s_lshr_b32 s89, s82, 8\n" \
  "s_add_u32 s89, s89, 1255424\n" \
  "s_add_u32 s53, s52, 0x4000000\n" \
  "s_add_u32 s55, s54, 0x4000000\n" \
  "s_lshl_b32 s72, s59, 8\n" \
  "s_lshl_b32 s83, s59, 10\n" \
  "s_lshl_b32 s73, s59, 13\n" \
  "s_add_u32 s73, s73, 131072\n" \
  "s_mov_b32 s65, 0x3f803f80\n" \
  "s_mov_b32 s68, 16384\n" \
  "s_mov_b32 s80, 0xaaaaaaaa\n" \
  "s_mov_b32 s81, 0xaaaaaaaa\n" \
  "v_mov_b32 v5, 0xff800000\n" \
  "v_add_u32 v44, s72, v0\n" \
  "v_mov_b32 v47, -1\n" \
  "v_add_u32 v44, 159744, v44\n" \
  "ds_write_b32 v44, v47 offset:0\n" \
  "ds_write_b32 v44, v47 offset:1024\n" \
  "ds_write_b32 v44, v47 offset:2048\n" \
  "ds_write_b32 v44, v47 offset:3072\n" \
  "s_add_u32 s60, s52, 1024\n" \
  "s_add_u32 m0, s69, 16384\n" \
  "s_nop 0\n" \
  "buffer_load_dwordx4 v1, s[40:43], s60 offen offset:0 nt lds\n" \
  "s_add_u32 s60, s52, 1024\n" \
  "s_add_u32 m0, s69, 17280\n" \
  "s_nop 0\n" \
  "buffer_load_dwordx4 v1, s[40:43], s60 offen offset:128 nt lds\n" \
  "s_add_u32 s60, s52, 1024\n" \
  "s_add_u32 m0, s69, 18176\n" \
  "s_nop 0\n" \
  "buffer_load_dwordx4 v1, s[40:43], s60 offen offset:256 nt lds\n" \
  "s_add_u32 s60, s52, 1024\n" \
  "s_add_u32 m0, s69, 19072\n" \
  "s_nop 0\n" \
  "buffer_load_dwordx4 v1, s[40:43], s60 offen offset:384 nt lds\n" \
  "s_add_u32 s60, s52, 1024\n" \
  "s_add_u32 m0, s69, 20480\n" \
  "s_nop 0\n" \
  "buffer_load_dwordx4 v2, s[44:47], s60 offen offset:0 nt lds\n" \
  "s_add_u32 s60, s52, 37888\n" \
  "s_add_u32 m0, s69, 21504\n" \
  "s_nop 0\n" \
  "buffer_load_dwordx4 v2, s[44:47], s60 offen offset:0 nt lds\n" \
  "s_add_u32 s60, s52, 1024\n" \
  "s_add_u32 m0, s69, 22272\n" \
  "s_nop 0\n" \
  "buffer_load_dwordx4 v2, s[44:47], s60 offen offset:256 nt lds\n" \
  "s_add_u32 s60, s52, 37888\n" \
  "s_add_u32 m0, s69, 23296\n" \
  "s_nop 0\n" \
  "buffer_load_dwordx4 v2, s[44:47], s60 offen offset:256 nt lds\n" \
  "s_mul_i32 s61, s59, 3072\n" \
  "s_add_u32 s61, s61, 147456\n" \
  "v_and_b32 v45, 1, v60\n" \
  "v_and_b32 v46, 2, v60\n" \
  "v_lshlrev_b32 v46, 3, v46\n" \
  "v_lshrrev_b32 v57, 1, v60\n" \
  "v_lshlrev_b32 v57, 2, v57\n" \
  "v_lshl_add_u32 v57, v61, 7, v57\n" \
  "v_add_u32 v57, s61, v57\n" \
  "v_lshl_add_u32 v56, v45, 10, v57\n" \
  "v_add_u32 v57, 0x800, v57\n" \
  "v_add_u32 v58, s72, v0\n" \
  "v_add_u32 v58, 147456, v58\n" \
  "v_mov_b32 v44, 0\n" \
  "v_mov_b32 v45, 0\n" \
  "v_mov_b32 v47, 0\n" \
  "v_mov_b32 v48, 0\n" \
  "v_mov_b32 v49, 0\n" \
  "v_mov_b32 v50, 0\n" \
  "v_mov_b32 v51, 0\n" \
  "v_mov_b32 v52, 0\n" \
  "v_mov_b32 v53, 0\n" \
  "v_mov_b32 v54, 0\n" \
  "v_mov_b32 v55, 0\n" \
  "v_mov_b32 v43, 0x3f80\n" \
  "v_lshlrev_b32 v43, v46, v43\n" \
  "v_mov_b32 v42, v46\n" \
  "v_mov_b32 v46, 0\n" \
  "v_mov_b32_dpp v44, v43 quad_perm:[0,1,2,3] row_mask:0xf bank_mask:0x1\n" \
  "v_mov_b32_dpp v45, v43 quad_perm:[0,1,2,3] row_mask:0xf bank_mask:0x2\n" \
  "v_mov_b32_dpp v46, v43 quad_perm:[0,1,2,3] row_mask:0xf bank_mask:0x4\n" \
  "v_mov_b32_dpp v47, v43 quad_perm:[0,1,2,3] row_mask:0xf bank_mask:0x8\n" \
  "s_waitcnt vmcnt(28)\n" \
  "v_lshlrev_b32 v40, v42, v40\n" \
  "v_lshlrev_b32 v41, v42, v41\n" \
  "s_nop 0\n" \
  "v_mov_b32_dpp v48, v40 quad_perm:[0,1,2,3] row_mask:0xf bank_mask:0x1\n" \
  "v_mov_b32_dpp v49, v40 quad_perm:[0,1,2,3] row_mask:0xf bank_mask:0x2\n" \
  "v_mov_b32_dpp v50, v40 quad_perm:[0,1,2,3] row_mask:0xf bank_mask:0x4\n" \
  "v_mov_b32_dpp v51, v40 quad_perm:[0,1,2,3] row_mask:0xf bank_mask:0x8\n" \
  "v_mov_b32_dpp v52, v41 quad_perm:[0,1,2,3] row_mask:0xf bank_mask:0x1\n" \
  "v_mov_b32_dpp v53, v41 quad_perm:[0,1,2,3] row_mask:0xf bank_mask:0x2\n" \
  "v_mov_b32_dpp v54, v41 quad_perm:[0,1,2,3] row_mask:0xf bank_mask:0x4\n" \
  "v_mov_b32_dpp v55, v41 quad_perm:[0,1,2,3] row_mask:0xf bank_mask:0x8\n" \
  "s_waitcnt vmcnt(24)\n" \
  "v_mfma_f32_16x16x32_bf16 v[24:27], v[8:11], v[48:51], 0\n" \
  "v_mfma_f32_16x16x32_bf16 v[32:35], v[8:11], v[44:47], 0\n" \
  "v_mfma_f32_16x16x32_bf16 v[28:31], v[12:15], v[48:51], 0\n" \
  "v_mfma_f32_16x16x32_bf16 v[36:39], v[12:15], v[44:47], 0\n" \
  "v_mfma_f32_16x16x32_bf16 v[24:27], v[16:19], v[52:55], v[24:27]\n" \
  "v_mfma_f32_16x16x32_bf16 v[32:35], v[16:19], v[44:47], v[32:35]\n" \
  "v_mfma_f32_16x16x32_bf16 v[28:31], v[20:23], v[52:55], v[28:31]\n" \
  "v_mfma_f32_16x16x32_bf16 v[36:39], v[20:23], v[44:47], v[36:39]\n" \
  "v_readfirstlane_b32 s88, v59\n" \
  "s_nop 3\n" \
  "s_mul_i32 s88, s88, 0x9e3779b1\n" \
  "s_add_u32 s88, s88, 0x7f4a7c15\n" \
  "ds_write2_b32 v56, v24, v25 offset0:0 offset1:8\n" \
  "ds_write2_b32 v56, v26, v27 offset0:16 offset1:24\n" \
  "ds_write2_b32 v56, v28, v29 offset0:128 offset1:136\n" \
  "ds_write2_b32 v56, v30, v31 offset0:144 offset1:152\n" \
  "ds_write2_b32 v57, v32, v33 offset0:0 offset1:8\n" \
  "ds_write2_b32 v57, v34, v35 offset0:16 offset1:24\n" \
  "ds_write2_b32 v57, v36, v37 offset0:128 offset1:136\n" \
  "ds_write2_b32 v57, v38, v39 offset0:144 offset1:152\n" \
  "s_waitcnt lgkmcnt(0)\n" \
  "s_barrier\n" \
  "ds_read2st64_b32 v[8:9], v58 offset0:0 offset1:4\n" \
  "ds_read2st64_b32 v[10:11], v58 offset0:12 offset1:16\n" \
  "ds_read2st64_b32 v[12:13], v58 offset0:24 offset1:28\n" \
  "ds_read2st64_b32 v[14:15], v58 offset0:36 offset1:40\n" \
  "ds_read2st64_b32 v[16:17], v58 offset0:8 offset1:20\n" \
  "ds_read2st64_b32 v[18:19], v58 offset0:32 offset1:44\n" \
  "v_lshlrev_b32 v55, 2, v0\n" \
  "s_add_u32 s60, %[cqo], s83\n" \
  "s_waitcnt lgkmcnt(0)\n" \
  "v_pk_add_f32 v[8:9], v[8:9], v[10:11]\n" \
  "v_pk_add_f32 v[12:13], v[12:13], v[14:15]\n" \
  "v_pk_add_f32 v[16:17], v[16:17], v[18:19]\n" \
  "v_pk_add_f32 v[8:9], v[8:9], v[12:13]\n" \
  "v_add_f32 v10, v16, v17\n" \
  "v_mov_b32 v11, s88\n" \
  "buffer_store_dwordx4 v[8:11], v55, s[48:51], s60 offen sc1\n" \
  "v_and_b32 v24, 2, v60\n" \
  "s_movk_i32 s61, 0x5555\n" \
  "v_mul_u32_u24 v24, s61, v24\n" \
  "v_add_u32 v24, 0x4444, v24\n" \
  "v_and_b32 v25, 3, v60\n" \
  "v_xor_b32 v26, 1, v25\n" \
  "v_lshl_add_u32 v25, v26, 2, v25\n" \
  "s_movk_i32 s61, 0x1111\n" \
  "v_mul_u32_u24 v25, s61, v25\n" \
  "v_lshl_add_u32 v4, v25, 16, v24\n" \
  "v_lshrrev_b32 v24, 2, v60\n" \
  "v_lshlrev_b32 v24, 4, v24\n" \
  "v_lshl_add_u32 v24, v61, 2, v24\n" \
  "v_and_b32 v25, 3, v60\n" \
  "v_add_u32 v24, v24, v25\n" \
  "v_lshlrev_b32 v53, 2, v24\n" \
  "s_lshl_b32 s92, s59, 7\n" \
  "s_add_u32 s57, s82, s92\n" \
  "s_nop 1\n" \
  "v_mov_b64 v[8:9], 0\n" \
  "v_mov_b64 v[10:11], 0\n" \
  "v_mov_b64 v[12:13], 0\n" \
  "v_mov_b64 v[14:15], 0\n" \
  "v_mov_b64 v[16:17], 0\n" \
  "v_mov_b64 v[18:19], 0\n" \
  "v_mov_b64 v[20:21], 0\n" \
  "v_mov_b64 v[22:23], 0\n" \
  "v_mov_b64 v[56:57], 0\n" \
  "v_mov_b64 v[58:59], 0\n" \
  "v_mov_b64 v[60:61], 0\n" \
  "v_mov_b64 v[62:63], 0\n" \
  "v_mov_b64 v[24:25], 0\n" \
  "v_mov_b64 v[26:27], 0\n" \
  "v_mov_b64 v[28:29], 0\n" \
  "v_mov_b64 v[30:31], 0\n" \
  "v_mov_b32 v6, v5\n" \
  "v_mov_b32 v7, 0\n" \
  "s_add_u32 s60, s52, 1536\n" \
  "s_add_u32 m0, s69, 24576\n" \
  "s_nop 0\n" \
  "buffer_load_dwordx4 v1, s[40:43], s60 offen offset:0 nt lds\n" \
  "s_add_u32 s60, s52, 1536\n" \
  "s_add_u32 m0, s69, 25472\n" \
  "s_nop 0\n" \
  "buffer_load_dwordx4 v1, s[40:43], s60 offen offset:128 nt lds\n" \
  "s_add_u32 s60, s52, 1536\n" \
  "s_add_u32 m0, s69, 26368\n" \
  "s_nop 0\n" \
  "buffer_load_dwordx4 v1, s[40:43], s60 offen offset:256 nt lds\n" \
  "s_add_u32 s60, s52, 1536\n" \
  "s_add_u32 m0, s69, 27264\n" \
  "s_nop 0\n" \
  "buffer_load_dwordx4 v1, s[40:43], s60 offen offset:384 nt lds\n" \
  "s_add_u32 s60, s52, 1536\n" \
  "s_add_u32 m0, s69, 28672\n" \
  "s_nop 0\n" \
  "buffer_load_dwordx4 v2, s[44:47], s60 offen offset:0 nt lds\n" \
  "s_add_u32 s60, s52, 38400\n" \
  "s_add_u32 m0, s69, 29696\n" \
  "s_nop 0\n" \
  "buffer_load_dwordx4 v2, s[44:47], s60 offen offset:0 nt lds\n" \
  "s_add_u32 s60, s52, 1536\n" \
  "s_add_u32 m0, s69, 30464\n" \
  "s_nop 0\n" \
  "buffer_load_dwordx4 v2, s[44:47], s60 offen offset:256 nt lds\n" \
  "s_add_u32 s60, s52, 38400\n" \
  "s_add_u32 m0, s69, 31488\n" \
  "s_nop 0\n" \
  "buffer_load_dwordx4 v2, s[44:47], s60 offen offset:256 nt lds\n" \
  "s_cmp_eq_u32 s56, 0\n" \
  "s_cbranch_scc1 LM_1_%=\n" \
  "v_and_b32 v54, 0x38, v0\n" \
  "v_lshlrev_b32 v54, 7, v54\n" \
  "v_sub_u32 v54, v1, v54\n" \
  "s_add_u32 s60, s54, 0\n" \
  "s_add_u32 m0, s73, 0\n" \
  "s_nop 0\n" \
  "buffer_load_dwordx4 v54, s[40:43], s60 offen offset:0 nt lds\n" \
  "v_and_b32 v54, 0x38, v0\n" \
  "v_lshlrev_b32 v54, 7, v54\n" \
  "v_sub_u32 v54, v1, v54\n" \
  "s_add_u32 s60, s54, 0\n" \
  "s_add_u32 m0, s73, 896\n" \
  "s_nop 0\n" \
  "buffer_load_dwordx4 v54, s[40:43], s60 offen offset:128 nt lds\n" \
  "v_and_b32 v54, 0x38, v0\n" \
  "v_lshlrev_b32 v54, 7, v54\n" \
  "v_sub_u32 v54, v1, v54\n" \
  "s_add_u32 s60, s54, 0\n" \
  "s_add_u32 m0, s73, 1792\n" \
  "s_nop 0\n" \
  "buffer_load_dwordx4 v54, s[40:43], s60 offen offset:256 nt lds\n" \
  "v_and_b32 v54, 0x38, v0\n" \
  "v_lshlrev_b32 v54, 7, v54\n" \
  "v_sub_u32 v54, v1, v54\n" \
  "s_add_u32 s60, s54, 0\n" \
  "s_add_u32 m0, s73, 2688\n" \
  "s_nop 0\n" \
  "buffer_load_dwordx4 v54, s[40:43], s60 offen offset:384 nt lds\n" \
  "v_and_b32 v54, 0xc0, v0\n" \
  "v_lshlrev_b32 v54, 4, v54\n" \
  "v_sub_u32 v54, v2, v54\n" \
  "s_add_u32 s60, s54, 0\n" \
  "s_add_u32 m0, s73, 4096\n" \
  "s_nop 0\n" \
  "buffer_load_dwordx4 v54, s[44:47], s60 offen offset:0 nt lds\n" \
  "v_and_b32 v54, 0xc0, v0\n" \
  "v_lshlrev_b32 v54, 4, v54\n" \
  "v_sub_u32 v54, v2, v54\n" \
  "s_add_u32 s60, s54, 32768\n" \
  "s_add_u32 m0, s73, 5120\n" \
  "s_nop 0\n" \
  "buffer_load_dwordx4 v54, s[44:47], s60 offen offset:0 nt lds\n" \
  "v_and_b32 v54, 0xc0, v0\n" \
  "v_lshlrev_b32 v54, 4, v54\n" \
  "v_sub_u32 v54, v2, v54\n" \
  "s_add_u32 s60, s54, 0\n" \
  "s_add_u32 m0, s73, 5888\n" \
  "s_nop 0\n" \
  "buffer_load_dwordx4 v54, s[44:47], s60 offen offset:256 nt lds\n" \
  "v_and_b32 v54, 0xc0, v0\n" \
  "v_lshlrev_b32 v54, 4, v54\n" \
  "v_sub_u32 v54, v2, v54\n" \
  "s_add_u32 s60, s54, 32768\n" \
  "s_add_u32 m0, s73, 6912\n" \
  "s_nop 0\n" \
  "buffer_load_dwordx4 v54, s[44:47], s60 offen offset:256 nt lds\n" \
  "LM_1_%=:\n" \
  "s_waitcnt vmcnt(24)\n" \
  "s_add_u32 s60, s52, 2048\n" \
  "buffer_load_dwordx4 a[0:3], v1, s[40:43], s60 offen offset:0 nt\n" \
  "s_add_u32 s60, s52, 2048\n" \
  "buffer_load_dwordx4 a[4:7], v1, s[40:43], s60 offen offset:128 nt\n" \
  "s_add_u32 s60, s52, 2048\n" \
  "buffer_load_dwordx4 a[8:11], v1, s[40:43], s60 offen offset:256 nt\n" \
  "s_add_u32 s60, s52, 2048\n" \
  "buffer_load_dwordx4 a[12:15], v1, s[40:43], s60 offen offset:384 nt\n" \
  "s_add_u32 s60, s52, 2048\n" \
  "buffer_load_dwordx4 a[16:19], v2, s[44:47], s60 offen offset:0 nt\n" \
  "s_add_u32 s60, s52, 38912\n" \
  "buffer_load_dwordx4 a[20:23], v2, s[44:47], s60 offen offset:0 nt\n" \
  "s_add_u32 s60, s52, 2048\n" \
  "buffer_load_dwordx4 a[24:27], v2, s[44:47], s60 offen offset:256 nt\n" \
  "s_add_u32 s60, s52, 38912\n" \
  "buffer_load_dwordx4 a[28:31], v2, s[44:47], s60 offen offset:256 nt\n" \
  "buffer_load_dword v52, off, s[48:51], s89 sc1\n" \
  "s_waitcnt vmcnt(25)\n" \
  "s_add_u32 s60, s52, 2560\n" \
  "buffer_load_dwordx4 a[32:35], v1, s[40:43], s60 offen offset:0 nt\n" \
  "s_add_u32 s60, s52, 2560\n" \
  "buffer_load_dwordx4 a[36:39], v1, s[40:43], s60 offen offset:128 nt\n" \
  "s_add_u32 s60, s52, 2560\n" \
  "buffer_load_dwordx4 a[40:43], v1, s[40:43], s60 offen offset:256 nt\n" \
  "s_add_u32 s60, s52, 2560\n" \
  "buffer_load_dwordx4 a[44:47], v1, s[40:43], s60 offen offset:384 nt\n" \
  "s_add_u32 s60, s52, 2560\n" \
  "buffer_load_dwordx4 a[48:51], v2, s[44:47], s60 offen offset:0 nt\n" \
  "s_add_u32 s60, s52, 39424\n" \
  "buffer_load_dwordx4 a[52:55], v2, s[44:47], s60 offen offset:0 nt\n" \
  "s_add_u32 s60, s52, 2560\n" \
  "buffer_load_dwordx4 a[56:59], v2, s[44:47], s60 offen offset:256 nt\n" \
  "s_add_u32 s60, s52, 39424\n" \
  "buffer_load_dwordx4 a[60:63], v2, s[44:47], s60 offen offset:256 nt\n" \
  "s_waitcnt vmcnt(25)\n" \
  "s_add_u32 s60, s52, 3072\n" \
  "buffer_load_dwordx4 a[64:67], v1, s[40:43], s60 offen offset:0 nt\n" \
  "s_add_u32 s60, s52, 3072\n" \
  "buffer_load_dwordx4 a[68:71], v1, s[40:43], s60 offen offset:128 nt\n" \
  "s_add_u32 s60, s52, 3072\n" \
  "buffer_load_dwordx4 a[72:75], v1, s[40:43], s60 offen offset:256 nt\n" \
  "s_add_u32 s60, s52, 3072\n" \
  "buffer_load_dwordx4 a[76:79], v1, s[40:43], s60 offen offset:384 nt\n" \
  "s_add_u32 s60, s52, 3072\n" \
  "buffer_load_dwordx4 a[80:83], v2, s[44:47], s60 offen offset:0 nt\n" \
  "s_add_u32 s60, s52, 39936\n" \
  "buffer_load_dwordx4 a[84:87], v2, s[44:47], s60 offen offset:0 nt\n" \
  "s_add_u32 s60, s52, 3072\n" \
  "buffer_load_dwordx4 a[88:91], v2, s[44:47], s60 offen offset:256 nt\n" \
  "s_add_u32 s60, s52, 39936\n" \
  "buffer_load_dwordx4 a[92:95], v2, s[44:47], s60 offen offset:256 nt\n" \
  "s_waitcnt vmcnt(25)\n" \
  "s_add_u32 s60, s52, 3584\n" \
  "buffer_load_dwordx4 a[96:99], v1, s[40:43], s60 offen offset:0 nt\n" \
  "s_add_u32 s60, s52, 3584\n" \
  "buffer_load_dwordx4 a[100:103], v1, s[40:43], s60 offen offset:128 nt\n" \
  "s_add_u32 s60, s52, 3584\n" \
  "buffer_load_dwordx4 a[104:107], v1, s[40:43], s60 offen offset:256 nt\n" \
  "s_add_u32 s60, s52, 3584\n" \
  "buffer_load_dwordx4 a[108:111], v1, s[40:43], s60 offen offset:384 nt\n" \
  "s_add_u32 s60, s52, 3584\n" \
  "buffer_load_dwordx4 a[112:115], v2, s[44:47], s60 offen offset:0 nt\n" \
  "s_add_u32 s60, s52, 40448\n" \
  "buffer_load_dwordx4 a[116:119], v2, s[44:47], s60 offen offset:0 nt\n" \
  "s_add_u32 s60, s52, 3584\n" \
  "buffer_load_dwordx4 a[120:123], v2, s[44:47], s60 offen offset:256 nt\n" \
  "s_add_u32 s60, s52, 40448\n" \
  "buffer_load_dwordx4 a[124:127], v2, s[44:47], s60 offen offset:256 nt\n" \
  "s_waitcnt vmcnt(24)\n" \
  "v_readfirstlane_b32 s61, v52\n" \
  "s_nop 3\n" \
  "s_cmp_eq_u32 s61, s88\n" \
  "s_cbranch_scc1 LM_2_%=\n" \
  "s_call_b64 s[66:67], LRW_%=\n" \
  "s_nop 1\n" \
  "LM_2_%=:\n" \
  "s_add_u32 m0, s92, 159744\n" \
  "s_add_u32 s60, s57, 0\n" \
  "s_mov_b32 exec_hi, 0\n" \
  "buffer_load_dword v0, s[48:51], s60 offen sc1 lds\n" \
  "s_mov_b32 exec_hi, -1\n" \
  "s_add_u32 m0, s92, 160768\n" \
  "s_add_u32 s60, s57, 16384\n" \
  "s_mov_b32 exec_hi, 0\n" \
  "buffer_load_dword v0, s[48:51], s60 offen sc1 lds\n" \
  "s_mov_b32 exec_hi, -1\n" \
  "s_add_u32 s60, s52, 4096\n" \
  "buffer_load_dwordx4 a[128:131], v1, s[40:43], s60 offen offset:0 nt\n" \
  "s_add_u32 s60, s52, 4096\n" \
  "buffer_load_dwordx4 a[132:135], v1, s[40:43], s60 offen offset:128 nt\n" \
  "s_add_u32 s60, s52, 4096\n" \
  "buffer_load_dwordx4 a[136:139], v1, s[40:43], s60 offen offset:256 nt\n" \
  "s_add_u32 s60, s52, 4096\n" \
  "buffer_load_dwordx4 a[140:143], v1, s[40:43], s60 offen offset:384 nt\n" \
  "s_add_u32 s60, s52, 4096\n" \
  "buffer_load_dwordx4 a[144:147], v2, s[44:47], s60 offen offset:0 nt\n" \
  "s_add_u32 s60, s52, 40960\n" \
  "buffer_load_dwordx4 a[148:151], v2, s[44:47], s60 offen offset:0 nt\n" \
  "s_add_u32 s60, s52, 4096\n" \
  "buffer_load_dwordx4 a[152:155], v2, s[44:47], s60 offen offset:256 nt\n" \
  "s_add_u32 s60, s52, 40960\n" \
  "buffer_load_dwordx4 a[156:159], v2, s[44:47], s60 offen offset:256 nt\n" \
  "s_add_u32 m0, s92, 159744\n" \
  "s_add_u32 s60, s57, 0\n" \
  "s_mov_b32 exec_hi, 0\n" \
  "buffer_load_dword v0, s[48:51], s60 offen sc1 lds\n" \
  "s_mov_b32 exec_hi, -1\n" \
  "s_add_u32 m0, s92, 160768\n" \
  "s_add_u32 s60, s57, 16384\n" \
  "s_mov_b32 exec_hi, 0\n" \
  "buffer_load_dword v0, s[48:51], s60 offen sc1 lds\n" \
  "s_mov_b32 exec_hi, -1\n" \
  "s_waitcnt vmcnt(28)\n" \
  "s_add_u32 s60, s52, 4608\n" \
  "buffer_load_dwordx4 a[160:163], v1, s[40:43], s60 offen offset:0 nt\n" \
  "s_add_u32 s60, s52, 4608\n" \
  "buffer_load_dwordx4 a[164:167], v1, s[40:43], s60 offen offset:128 nt\n" \
  "s_add_u32 s60, s52, 4608\n" \
  "buffer_load_dwordx4 a[168:171], v1, s[40:43], s60 offen offset:256 nt\n" \
  "s_add_u32 s60, s52, 4608\n" \
  "buffer_load_dwordx4 a[172:175], v1, s[40:43], s60 offen offset:384 nt\n" \
  "s_add_u32 s60, s52, 4608\n" \
  "buffer_load_dwordx4 a[176:179], v2, s[44:47], s60 offen offset:0 nt\n" \
  "s_add_u32 s60, s52, 41472\n" \
  "buffer_load_dwordx4 a[180:183], v2, s[44:47], s60 offen offset:0 nt\n" \
  "s_add_u32 s60, s52, 4608\n" \
  "buffer_load_dwordx4 a[184:187], v2, s[44:47], s60 offen offset:256 nt\n" \
  "s_add_u32 s60, s52, 41472\n" \
  "buffer_load_dwordx4 a[188:191], v2, s[44:47], s60 offen offset:256 nt\n" \
  "s_add_u32 m0, s92, 159744\n" \
  "s_add_u32 s60, s57, 0\n" \
  "s_mov_b32 exec_hi, 0\n" \
  "buffer_load_dword v0, s[48:51], s60 offen sc1 lds\n" \
  "s_mov_b32 exec_hi, -1\n" \
  "s_add_u32 m0, s92, 160768\n" \
  "s_add_u32 s60, s57, 16384\n" \
  "s_mov_b32 exec_hi, 0\n" \
  "buffer_load_dword v0, s[48:51], s60 offen sc1 lds\n" \
  "s_mov_b32 exec_hi, -1\n" \
  "s_waitcnt vmcnt(30)\n" \
  "s_add_u32 s60, s52, 5120\n" \
  "buffer_load_dwordx4 a[192:195], v1, s[40:43], s60 offen offset:0 nt\n" \
  "s_add_u32 s60, s52, 5120\n" \
  "buffer_load_dwordx4 a[196:199], v1, s[40:43], s60 offen offset:128 nt\n" \
  "s_add_u32 s60, s52, 5120\n" \
  "buffer_load_dwordx4 a[200:203], v1, s[40:43], s60 offen offset:256 nt\n" \
  "s_add_u32 s60, s52, 5120\n" \
  "buffer_load_dwordx4 a[204:207], v1, s[40:43], s60 offen offset:384 nt\n" \
  "s_add_u32 s60, s52, 5120\n" \
  "buffer_load_dwordx4 a[208:211], v2, s[44:47], s60 offen offset:0 nt\n" \
  "s_add_u32 s60, s52, 41984\n" \
  "buffer_load_dwordx4 a[212:215], v2, s[44:47], s60 offen offset:0 nt\n" \
  "s_add_u32 s60, s52, 5120\n" \
  "buffer_load_dwordx4 a[216:219], v2, s[44:47], s60 offen offset:256 nt\n" \
  "s_add_u32 s60, s52, 41984\n" \
  "buffer_load_dwordx4 a[220:223], v2, s[44:47], s60 offen offset:256 nt\n" \
  "s_add_u32 m0, s92, 159744\n" \
  "s_add_u32 s60, s57, 0\n" \
  "s_mov_b32 exec_hi, 0\n" \
  "buffer_load_dword v0, s[48:51], s60 offen sc1 lds\n" \
  "s_mov_b32 exec_hi, -1\n" \
  "s_add_u32 m0, s92, 160768\n" \
  "s_add_u32 s60, s57, 16384\n" \
  "s_mov_b32 exec_hi, 0\n" \
  "buffer_load_dword v0, s[48:51], s60 offen sc1 lds\n" \
  "s_mov_b32 exec_hi, -1\n" \
  "s_waitcnt vmcnt(32)\n" \
  "s_add_u32 s60, s52, 5632\n" \
  "buffer_load_dwordx4 a[224:227], v1, s[40:43], s60 offen offset:0 nt\n" \
  "s_add_u32 s60, s52, 5632\n" \
  "buffer_load_dwordx4 a[228:231], v1, s[40:43], s60 offen offset:128 nt\n" \
  "s_add_u32 s60, s52, 5632\n" \
  "buffer_load_dwordx4 a[232:235], v1, s[40:43], s60 offen offset:256 nt\n" \
  "s_add_u32 s60, s52, 5632\n" \
  "buffer_load_dwordx4 a[236:239], v1, s[40:43], s60 offen offset:384 nt\n" \
  "s_add_u32 s60, s52, 5632\n" \
  "buffer_load_dwordx4 a[240:243], v2, s[44:47], s60 offen offset:0 nt\n" \
  "s_add_u32 s60, s52, 42496\n" \
  "buffer_load_dwordx4 a[244:247], v2, s[44:47], s60 offen offset:0 nt\n" \
  "s_add_u32 s60, s52, 5632\n" \
  "buffer_load_dwordx4 a[248:251], v2, s[44:47], s60 offen offset:256 nt\n" \
  "s_add_u32 s60, s52, 42496\n" \
  "buffer_load_dwordx4 a[252:255], v2, s[44:47], s60 offen offset:256 nt\n" \
  "s_add_u32 m0, s92, 159744\n" \
  "s_add_u32 s60, s57, 0\n" \
  "s_mov_b32 exec_hi, 0\n" \
  "buffer_load_dword v0, s[48:51], s60 offen sc1 lds\n" \
  "s_mov_b32 exec_hi, -1\n" \
  "s_add_u32 m0, s92, 160768\n" \
  "s_add_u32 s60, s57, 16384\n" \
  "s_mov_b32 exec_hi, 0\n" \
  "buffer_load_dword v0, s[48:51], s60 offen sc1 lds\n" \
  "s_mov_b32 exec_hi, -1\n" \
  "s_waitcnt vmcnt(32)\n" \
  "s_add_u32 s60, s52, 6144\n" \
  "buffer_load_dwordx4 v[64:67], v1, s[40:43], s60 offen offset:0 nt\n" \
  "s_add_u32 s60, s52, 6144\n" \
  "buffer_load_dwordx4 v[68:71], v1, s[40:43], s60 offen offset:128 nt\n" \
  "s_add_u32 s60, s52, 6144\n" \
  "buffer_load_dwordx4 v[72:75], v1, s[40:43], s60 offen offset:256 nt\n" \
  "s_add_u32 s60, s52, 6144\n" \
  "buffer_load_dwordx4 v[76:79], v1, s[40:43], s60 offen offset:384 nt\n" \
  "s_add_u32 s60, s52, 6144\n" \
  "buffer_load_dwordx4 v[80:83], v2, s[44:47], s60 offen offset:0 nt\n" \
  "s_add_u32 s60, s52, 43008\n" \
  "buffer_load_dwordx4 v[84:87], v2, s[44:47], s60 offen offset:0 nt\n" \
  "s_add_u32 s60, s52, 6144\n" \
  "buffer_load_dwordx4 v[88:91], v2, s[44:47], s60 offen offset:256 nt\n" \
  "s_add_u32 s60, s52, 43008\n" \
  "buffer_load_dwordx4 v[92:95], v2, s[44:47], s60 offen offset:256 nt\n" \
  "s_add_u32 m0, s92, 159744\n" \
  "s_add_u32 s60, s57, 0\n" \
  "s_mov_b32 exec_hi, 0\n" \
  "buffer_load_dword v0, s[48:51], s60 offen sc1 lds\n" \
  "s_mov_b32 exec_hi, -1\n" \
  "s_add_u32 m0, s92, 160768\n" \
  "s_add_u32 s60, s57, 16384\n" \
  "s_mov_b32 exec_hi, 0\n" \
  "buffer_load_dword v0, s[48:51], s60 offen sc1 lds\n" \
  "s_mov_b32 exec_hi, -1\n" \
  "s_waitcnt vmcnt(32)\n" \
  "s_add_u32 s60, s52, 6656\n" \
  "buffer_load_dwordx4 v[96:99], v1, s[40:43], s60 offen offset:0 nt\n" \
  "s_add_u32 s60, s52, 6656\n" \
  "buffer_load_dwordx4 v[100:103], v1, s[40:43], s60 offen offset:128 nt\n" \
  "s_add_u32 s60, s52, 6656\n" \
  "buffer_load_dwordx4 v[104:107], v1, s[40:43], s60 offen offset:256 nt\n" \
  "s_add_u32 s60, s52, 6656\n" \
  "buffer_load_dwordx4 v[108:111], v1, s[40:43], s60 offen offset:384 nt\n" \
  "s_add_u32 s60, s52, 6656\n" \
  "buffer_load_dwordx4 v[112:115], v2, s[44:47], s60 offen offset:0 nt\n" \
  "s_add_u32 s60, s52, 43520\n" \
  "buffer_load_dwordx4 v[116:119], v2, s[44:47], s60 offen offset:0 nt\n" \
  "s_add_u32 s60, s52, 6656\n" \
  "buffer_load_dwordx4 v[120:123], v2, s[44:47], s60 offen offset:256 nt\n" \
  "s_add_u32 s60, s52, 43520\n" \
  "buffer_load_dwordx4 v[124:127], v2, s[44:47], s60 offen offset:256 nt\n" \
  "s_add_u32 m0, s92, 159744\n" \
  "s_add_u32 s60, s57, 0\n" \
  "s_mov_b32 exec_hi, 0\n" \
  "buffer_load_dword v0, s[48:51], s60 offen sc1 lds\n" \
  "s_mov_b32 exec_hi, -1\n" \
  "s_add_u32 m0, s92, 160768\n" \
  "s_add_u32 s60, s57, 16384\n" \
  "s_mov_b32 exec_hi, 0\n" \
  "buffer_load_dword v0, s[48:51], s60 offen sc1 lds\n" \
  "s_mov_b32 exec_hi, -1\n" \
  "s_waitcnt vmcnt(32)\n" \
  "s_add_u32 s60, s52, 7168\n" \
  "buffer_load_dwordx4 v[128:131], v1, s[40:43], s60 offen offset:0 nt\n" \
  "s_add_u32 s60, s52, 7168\n" \
  "buffer_load_dwordx4 v[132:135], v1, s[40:43], s60 offen offset:128 nt\n" \
  "s_add_u32 s60, s52, 7168\n" \
  "buffer_load_dwordx4 v[136:139], v1, s[40:43], s60 offen offset:256 nt\n" \
  "s_add_u32 s60, s52, 7168\n" \
  "buffer_load_dwordx4 v[140:143], v1, s[40:43], s60 offen offset:384 nt\n" \
  "s_add_u32 s60, s52, 7168\n" \
  "buffer_load_dwordx4 v[144:147], v2, s[44:47], s60 offen offset:0 nt\n" \
  "s_add_u32 s60, s52, 44032\n" \
  "buffer_load_dwordx4 v[148:151], v2, s[44:47], s60 offen offset:0 nt\n" \
  "s_add_u32 s60, s52, 7168\n" \
  "buffer_load_dwordx4 v[152:155], v2, s[44:47], s60 offen offset:256 nt\n" \
  "s_add_u32 s60, s52, 44032\n" \
  "buffer_load_dwordx4 v[156:159], v2, s[44:47], s60 offen offset:256 nt\n" \
  "s_add_u32 m0, s92, 159744\n" \
  "s_add_u32 s60, s57, 0\n" \
  "s_mov_b32 exec_hi, 0\n" \
  "buffer_load_dword v0, s[48:51], s60 offen sc1 lds\n" \
  "s_mov_b32 exec_hi, -1\n" \
  "s_add_u32 m0, s92, 160768\n" \
  "s_add_u32 s60, s57, 16384\n" \
  "s_mov_b32 exec_hi, 0\n" \
  "buffer_load_dword v0, s[48:51], s60 offen sc1 lds\n" \
  "s_mov_b32 exec_hi, -1\n" \
  "s_waitcnt vmcnt(32)\n" \
  "s_add_u32 s60, s52, 7680\n" \
  "buffer_load_dwordx4 v[160:163], v1, s[40:43], s60 offen offset:0 nt\n" \
  "s_add_u32 s60, s52, 7680\n" \
  "buffer_load_dwordx4 v[164:167], v1, s[40:43], s60 offen offset:128 nt\n" \
  "s_add_u32 s60, s52, 7680\n" \
  "buffer_load_dwordx4 v[168:171], v1, s[40:43], s60 offen offset:256 nt\n" \
  "s_add_u32 s60, s52, 7680\n" \
  "buffer_load_dwordx4 v[172:175], v1, s[40:43], s60 offen offset:384 nt\n" \
  "s_add_u32 s60, s52, 7680\n" \
  "buffer_load_dwordx4 v[176:179], v2, s[44:47], s60 offen offset:0 nt\n" \
  "s_add_u32 s60, s52, 44544\n" \
  "buffer_load_dwordx4 v[180:183], v2, s[44:47], s60 offen offset:0 nt\n" \
  "s_add_u32 s60, s52, 7680\n" \
  "buffer_load_dwordx4 v[184:187], v2, s[44:47], s60 offen offset:256 nt\n" \
  "s_add_u32 s60, s52, 44544\n" \
  "buffer_load_dwordx4 v[188:191], v2, s[44:47], s60 offen offset:256 nt\n" \
  "s_add_u32 m0, s92, 159744\n" \
  "s_add_u32 s60, s57, 0\n" \
  "s_mov_b32 exec_hi, 0\n" \
  "buffer_load_dword v0, s[48:51], s60 offen sc1 lds\n" \
  "s_mov_b32 exec_hi, -1\n" \
  "s_add_u32 m0, s92, 160768\n" \
  "s_add_u32 s60, s57, 16384\n" \
  "s_mov_b32 exec_hi, 0\n" \
  "buffer_load_dword v0, s[48:51], s60 offen sc1 lds\n" \
  "s_mov_b32 exec_hi, -1\n" \
  "s_mov_b32 s70, 159744\n" \
  "s_add_u32 s71, s82, 0\n" \
  "s_waitcnt lgkmcnt(0)\n" \
  "s_call_b64 s[66:67], LQL_%=\n" \
  "s_nop 1\n" \
  "ds_read_b128 v[192:195], v3 offset:0\n" \
  "ds_read_b128 v[196:199], v3 offset:1024\n" \
  "ds_read_b128 v[200:203], v3 offset:2048\n" \
  "ds_read_b128 v[204:207], v3 offset:3072\n" \
  "ds_read_b128 v[208:211], v3 offset:4096\n" \
  "ds_read_b128 v[212:215], v3 offset:5120\n" \
  "ds_read_b128 v[216:219], v3 offset:6144\n" \
  "ds_read_b128 v[220:223], v3 offset:7168\n" \
  "ds_read_b128 v[224:227], v3 offset:8192\n" \
  "ds_read_b128 v[228:231], v3 offset:9216\n" \
  "ds_read_b128 v[232:235], v3 offset:10240\n" \
  "ds_read_b128 v[236:239], v3 offset:11264\n" \
  "ds_read_b128 v[240:243], v3 offset:12288\n" \
  "ds_read_b128 v[244:247], v3 offset:13312\n" \
  "ds_read_b128 v[248:251], v3 offset:14336\n" \
  "ds_read_b128 v[252:255], v3 offset:15360\n" \
  "s_waitcnt lgkmcnt(8)\n" \
  "s_mov_b32 s62, 0xaaaa\n" \
  "s_mov_b32 s63, 0x0\n" \
  "s_call_b64 s[66:67], LCW0_%=\n" \
  "s_nop 1\n" \
  "ds_read_b128 v[192:195], v3 offset:16384\n" \
  "ds_read_b128 v[196:199], v3 offset:17408\n" \
  "ds_read_b128 v[200:203], v3 offset:18432\n" \
  "ds_read_b128 v[204:207], v3 offset:19456\n" \
  "ds_read_b128 v[208:211], v3 offset:20480\n" \
  "ds_read_b128 v[212:215], v3 offset:21504\n" \
  "ds_read_b128 v[216:219], v3 offset:22528\n" \
  "ds_read_b128 v[220:223], v3 offset:23552\n" \
  "s_waitcnt lgkmcnt(8)\n" \
  "s_mov_b32 s62, 0xaaaa\n" \
  "s_mov_b32 s63, 0x0\n" \
  "s_call_b64 s[66:67], LCW1_%=\n" \
  "s_nop 1\n" \
  "ds_read_b128 v[224:227], v3 offset:24576\n" \
  "ds_read_b128 v[228:231], v3 offset:25600\n" \
  "ds_read_b128 v[232:235], v3 offset:26624\n" \
  "ds_read_b128 v[236:239], v3 offset:27648\n" \
  "ds_read_b128 v[240:243], v3 offset:28672\n" \
  "ds_read_b128 v[244:247], v3 offset:29696\n" \
  "ds_read_b128 v[248:251], v3 offset:30720\n" \
  "ds_read_b128 v[252:255], v3 offset:31744\n" \
  "s_waitcnt lgkmcnt(8)\n" \
  "s_mov_b32 s62, 0xaaaa\n" \
  "s_mov_b32 s63, 0x0\n" \
  "s_call_b64 s[66:67], LCW0_%=\n" \
  "s_nop 1\n" \
  "s_cmp_eq_u32 s56, 0\n" \
  "s_cbranch_scc1 LM_3_%=\n" \
  "v_lshlrev_b32 v55, 2, v0\n" \
  "v_add_u32 v55, s73, v55\n" \
  "ds_read_b128 v[192:195], v55 offset:0\n" \
  "ds_read_b128 v[196:199], v55 offset:1024\n" \
  "ds_read_b128 v[200:203], v55 offset:2048\n" \
  "ds_read_b128 v[204:207], v55 offset:3072\n" \
  "ds_read_b128 v[208:211], v55 offset:4096\n" \
  "ds_read_b128 v[212:215], v55 offset:5120\n" \
  "ds_read_b128 v[216:219], v55 offset:6144\n" \
  "ds_read_b128 v[220:223], v55 offset:7168\n" \
  "LM_3_%=:\n" \
  "s_waitcnt lgkmcnt(0)\n" \
  "s_mov_b32 s62, 0xaaaa\n" \
  "s_mov_b32 s63, 0x0\n" \
  "s_call_b64 s[66:67], LCW1_%=\n" \
  "s_nop 1\n" \
  "s_cmp_eq_u32 s56, 0\n" \
  "s_cbranch_scc1 LM_4_%=\n" \
  "s_mov_b32 s62, 0xaaaa\n" \
  "s_mov_b32 s63, 0x0\n" \
  "s_call_b64 s[66:67], LCW0_%=\n" \
  "s_nop 1\n" \
  "LM_4_%=:\n" \
  "s_waitcnt vmcnt(32)\n" \
  "s_add_u32 s60, s52, 8192\n" \
  "buffer_load_dwordx4 v[192:195], v1, s[40:43], s60 offen offset:0 \n" \
  "s_add_u32 s60, s52, 8192\n" \
  "buffer_load_dwordx4 v[196:199], v1, s[40:43], s60 offen offset:128 \n" \
  "s_add_u32 s60, s52, 8192\n" \
  "buffer_load_dwordx4 v[200:203], v1, s[40:43], s60 offen offset:256 \n" \
  "s_add_u32 s60, s52, 8192\n" \
  "buffer_load_dwordx4 v[204:207], v1, s[40:43], s60 offen offset:384 \n" \
  "s_add_u32 s60, s52, 8192\n" \
  "buffer_load_dwordx4 v[208:211], v2, s[44:47], s60 offen offset:0 \n" \
  "s_add_u32 s60, s52, 45056\n" \
  "buffer_load_dwordx4 v[212:215], v2, s[44:47], s60 offen offset:0 \n" \
  "s_add_u32 s60, s52, 8192\n" \
  "buffer_load_dwordx4 v[216:219], v2, s[44:47], s60 offen offset:256 \n" \
  "s_add_u32 s60, s52, 45056\n" \
  "buffer_load_dwordx4 v[220:223], v2, s[44:47], s60 offen offset:256 \n" \
  "s_mov_b32 s62, 0xaaaaaaaa\n" \
  "s_mov_b32 s63, 0xaaaaaaaa\n" \
  "s_call_b64 s[66:67], LC40_%=\n" \
  "s_nop 1\n" \
  "s_mov_b32 s62, 0xaaaaaaaa\n" \
  "s_mov_b32 s63, 0xaaaaaaaa\n" \
  "s_call_b64 s[66:67], LC41_%=\n" \
  "s_nop 1\n" \
  "s_waitcnt vmcnt(30)\n" \
  "s_add_u32 s60, s52, 8704\n" \
  "buffer_load_dwordx4 v[224:227], v1, s[40:43], s60 offen offset:0 \n" \
  "s_add_u32 s60, s52, 8704\n" \
  "buffer_load_dwordx4 v[228:231], v1, s[40:43], s60 offen offset:128 \n" \
  "s_add_u32 s60, s52, 8704\n" \
  "buffer_load_dwordx4 v[232:235], v1, s[40:43], s60 offen offset:256 \n" \
  "s_add_u32 s60, s52, 8704\n" \
  "buffer_load_dwordx4 v[236:239], v1, s[40:43], s60 offen offset:384 \n" \
  "s_add_u32 s60, s52, 8704\n" \
  "buffer_load_dwordx4 v[240:243], v2, s[44:47], s60 offen offset:0 \n" \
  "s_add_u32 s60, s52, 45568\n" \
  "buffer_load_dwordx4 v[244:247], v2, s[44:47], s60 offen offset:0 \n" \
  "s_add_u32 s60, s52, 8704\n" \
  "buffer_load_dwordx4 v[248:251], v2, s[44:47], s60 offen offset:256 \n" \
  "s_add_u32 s60, s52, 45568\n" \
  "buffer_load_dwordx4 v[252:255], v2, s[44:47], s60 offen offset:256 \n" \
  "s_waitcnt vmcnt(18)\n" \
  "s_mov_b32 s62, 0xaaaaaaaa\n" \
  "s_mov_b32 s63, 0xaaaaaaaa\n" \
  "s_call_b64 s[66:67], LC42_%=\n" \
  "s_nop 1\n" \
  "s_waitcnt vmcnt(8)\n" \
  "s_mov_b32 s62, 0xaaaa\n" \
  "s_mov_b32 s63, 0x0\n" \
  "s_call_b64 s[66:67], LCW0_%=\n" \
  "s_nop 1\n" \
  "s_add_u32 s60, s53, 0\n" \
  "buffer_load_dwordx4 v[192:195], v1, s[40:43], s60 offen offset:0 nt\n" \
  "s_add_u32 s60, s53, 0\n" \
  "buffer_load_dwordx4 v[196:199], v1, s[40:43], s60 offen offset:128 nt\n" \
  "s_add_u32 s60, s53, 0\n" \
  "buffer_load_dwordx4 v[200:203], v1, s[40:43], s60 offen offset:256 nt\n" \
  "s_add_u32 s60, s53, 0\n" \
  "buffer_load_dwordx4 v[204:207], v1, s[40:43], s60 offen offset:384 nt\n" \
  "s_add_u32 s60, s53, 0\n" \
  "buffer_load_dwordx4 v[208:211], v2, s[44:47], s60 offen offset:0 nt\n" \
  "s_add_u32 s60, s53, 36864\n" \
  "buffer_load_dwordx4 v[212:215], v2, s[44:47], s60 offen offset:0 nt\n" \
  "s_add_u32 s60, s53, 0\n" \
  "buffer_load_dwordx4 v[216:219], v2, s[44:47], s60 offen offset:256 nt\n" \
  "s_add_u32 s60, s53, 36864\n" \
  "buffer_load_dwordx4 v[220:223], v2, s[44:47], s60 offen offset:256 nt\n" \
  "s_waitcnt vmcnt(8)\n" \
  "s_mov_b32 s62, 0xaaaa\n" \
  "s_mov_b32 s63, 0x0\n" \
  "s_call_b64 s[66:67], LCW1_%=\n" \
  "s_nop 1\n" \
  "s_add_u32 s60, s53, 512\n" \
  "buffer_load_dwordx4 v[224:227], v1, s[40:43], s60 offen offset:0 nt\n" \
  "s_add_u32 s60, s53, 512\n" \
  "buffer_load_dwordx4 v[228:231], v1, s[40:43], s60 offen offset:128 nt\n" \
  "s_add_u32 s60, s53, 512\n" \
  "buffer_load_dwordx4 v[232:235], v1, s[40:43], s60 offen offset:256 nt\n" \
  "s_add_u32 s60, s53, 512\n" \
  "buffer_load_dwordx4 v[236:239], v1, s[40:43], s60 offen offset:384 nt\n" \
  "s_add_u32 s60, s53, 512\n" \
  "buffer_load_dwordx4 v[240:243], v2, s[44:47], s60 offen offset:0 nt\n" \
  "s_add_u32 s60, s53, 37376\n" \
  "buffer_load_dwordx4 v[244:247], v2, s[44:47], s60 offen offset:0 nt\n" \
  "s_add_u32 s60, s53, 512\n" \
  "buffer_load_dwordx4 v[248:251], v2, s[44:47], s60 offen offset:256 nt\n" \
  "s_add_u32 s60, s53, 37376\n" \
  "buffer_load_dwordx4 v[252:255], v2, s[44:47], s60 offen offset:256 nt\n" \
  "s_add_u32 s70, s58, 0\n" \
  "s_mov_b32 s71, 147456\n" \
  "s_mov_b32 s77, 155648\n" \
  "s_waitcnt lgkmcnt(0)\n" \
  "s_call_b64 s[66:67], LDU_%=\n" \
  "s_nop 1\n" \
  "v_mov_b64 v[24:25], 0\n" \
  "v_mov_b64 v[26:27], 0\n" \
  "v_mov_b64 v[28:29], 0\n" \
  "v_mov_b64 v[30:31], 0\n" \
  "v_mov_b32 v6, v5\n" \
  "v_mov_b32 v7, 0\n" \
  "s_mov_b32 s70, 160768\n" \
  "s_add_u32 s71, s82, 16384\n" \
  "s_waitcnt lgkmcnt(0)\n" \
  "s_call_b64 s[66:67], LQL_%=\n" \
  "s_nop 1\n" \
  "s_waitcnt vmcnt(8)\n" \
  "s_mov_b32 s62, 0xaaaa\n" \
  "s_mov_b32 s63, 0x0\n" \
  "s_call_b64 s[66:67], LCW0_%=\n" \
  "s_nop 1\n" \
  "s_waitcnt vmcnt(0)\n" \
  "s_add_u32 s60, s53, 1024\n" \
  "buffer_load_dwordx4 v[192:195], v1, s[40:43], s60 offen offset:0 nt\n" \
  "s_add_u32 s60, s53, 1024\n" \
  "buffer_load_dwordx4 v[196:199], v1, s[40:43], s60 offen offset:128 nt\n" \
  "s_add_u32 s60, s53, 1024\n" \
  "buffer_load_dwordx4 v[200:203], v1, s[40:43], s60 offen offset:256 nt\n" \
  "s_add_u32 s60, s53, 1024\n" \
  "buffer_load_dwordx4 v[204:207], v1, s[40:43], s60 offen offset:384 nt\n" \
  "s_add_u32 s60, s53, 1024\n" \
  "buffer_load_dwordx4 v[208:211], v2, s[44:47], s60 offen offset:0 nt\n" \
  "s_add_u32 s60, s53, 37888\n" \
  "buffer_load_dwordx4 v[212:215], v2, s[44:47], s60 offen offset:0 nt\n" \
  "s_add_u32 s60, s53, 1024\n" \
  "buffer_load_dwordx4 v[216:219], v2, s[44:47], s60 offen offset:256 nt\n" \
  "s_add_u32 s60, s53, 37888\n" \
  "buffer_load_dwordx4 v[220:223], v2, s[44:47], s60 offen offset:256 nt\n" \
  "s_mov_b32 s62, 0xaaaa\n" \
  "s_mov_b32 s63, 0x0\n" \
  "s_call_b64 s[66:67], LCW1_%=\n" \
  "s_nop 1\n" \
  "s_waitcnt vmcnt(0)\n" \
  "s_add_u32 s60, s53, 1536\n" \
  "buffer_load_dwordx4 v[224:227], v1, s[40:43], s60 offen offset:0 nt\n" \
  "s_add_u32 s60, s53, 1536\n" \
  "buffer_load_dwordx4 v[228:231], v1, s[40:43], s60 offen offset:128 nt\n" \
  "s_add_u32 s60, s53, 1536\n" \
  "buffer_load_dwordx4 v[232:235], v1, s[40:43], s60 offen offset:256 nt\n" \
  "s_add_u32 s60, s53, 1536\n" \
  "buffer_load_dwordx4 v[236:239], v1, s[40:43], s60 offen offset:384 nt\n" \
  "s_add_u32 s60, s53, 1536\n" \
  "buffer_load_dwordx4 v[240:243], v2, s[44:47], s60 offen offset:0 nt\n" \
  "s_add_u32 s60, s53, 38400\n" \
  "buffer_load_dwordx4 v[244:247], v2, s[44:47], s60 offen offset:0 nt\n" \
  "s_add_u32 s60, s53, 1536\n" \
  "buffer_load_dwordx4 v[248:251], v2, s[44:47], s60 offen offset:256 nt\n" \
  "s_add_u32 s60, s53, 38400\n" \
  "buffer_load_dwordx4 v[252:255], v2, s[44:47], s60 offen offset:256 nt\n" \
  "s_mov_b32 s62, 0xaaaa\n" \
  "s_mov_b32 s63, 0x0\n" \
  "s_call_b64 s[66:67], LCW0_%=\n" \
  "s_nop 1\n" \
  "s_waitcnt vmcnt(0)\n" \
  "s_add_u32 s60, s53, 2048\n" \
  "buffer_load_dwordx4 v[192:195], v1, s[40:43], s60 offen offset:0 nt\n" \
  "s_add_u32 s60, s53, 2048\n" \
  "buffer_load_dwordx4 v[196:199], v1, s[40:43], s60 offen offset:128 nt\n" \
  "s_add_u32 s60, s53, 2048\n" \
  "buffer_load_dwordx4 v[200:203], v1, s[40:43], s60 offen offset:256 nt\n" \
  "s_add_u32 s60, s53, 2048\n" \
  "buffer_load_dwordx4 v[204:207], v1, s[40:43], s60 offen offset:384 nt\n" \
  "s_add_u32 s60, s53, 2048\n" \
  "buffer_load_dwordx4 v[208:211], v2, s[44:47], s60 offen offset:0 nt\n" \
  "s_add_u32 s60, s53, 38912\n" \
  "buffer_load_dwordx4 v[212:215], v2, s[44:47], s60 offen offset:0 nt\n" \
  "s_add_u32 s60, s53, 2048\n" \
  "buffer_load_dwordx4 v[216:219], v2, s[44:47], s60 offen offset:256 nt\n" \
  "s_add_u32 s60, s53, 38912\n" \
  "buffer_load_dwordx4 v[220:223], v2, s[44:47], s60 offen offset:256 nt\n" \
  "s_mov_b32 s62, 0xaaaa\n" \
  "s_mov_b32 s63, 0x0\n" \
  "s_call_b64 s[66:67], LCW1_%=\n" \
  "s_nop 1\n" \
  "s_waitcnt vmcnt(0)\n" \
  "s_add_u32 s60, s53, 2560\n" \
  "buffer_load_dwordx4 v[224:227], v1, s[40:43], s60 offen offset:0 nt\n" \
  "s_add_u32 s60, s53, 2560\n" \
  "buffer_load_dwordx4 v[228:231], v1, s[40:43], s60 offen offset:128 nt\n" \
  "s_add_u32 s60, s53, 2560\n" \
  "buffer_load_dwordx4 v[232:235], v1, s[40:43], s60 offen offset:256 nt\n" \
  "s_add_u32 s60, s53, 2560\n" \
  "buffer_load_dwordx4 v[236:239], v1, s[40:43], s60 offen offset:384 nt\n" \
  "s_add_u32 s60, s53, 2560\n" \
  "buffer_load_dwordx4 v[240:243], v2, s[44:47], s60 offen offset:0 nt\n" \
  "s_add_u32 s60, s53, 39424\n" \
  "buffer_load_dwordx4 v[244:247], v2, s[44:47], s60 offen offset:0 nt\n" \
  "s_add_u32 s60, s53, 2560\n" \
  "buffer_load_dwordx4 v[248:251], v2, s[44:47], s60 offen offset:256 nt\n" \
  "s_add_u32 s60, s53, 39424\n" \
  "buffer_load_dwordx4 v[252:255], v2, s[44:47], s60 offen offset:256 nt\n" \
  "s_add_u32 m0, s92, 161792\n" \
  "s_add_u32 s60, s57, 32768\n" \
  "s_mov_b32 exec_hi, 0\n" \
  "buffer_load_dword v0, s[48:51], s60 offen sc1 lds\n" \
  "s_mov_b32 exec_hi, -1\n" \
  "s_mov_b32 s62, 0xaaaa\n" \
  "s_mov_b32 s63, 0x0\n" \
  "s_call_b64 s[66:67], LCW0_%=\n" \
  "s_nop 1\n" \
  "s_waitcnt vmcnt(1)\n" \
  "s_add_u32 s60, s53, 3072\n" \
  "buffer_load_dwordx4 v[192:195], v1, s[40:43], s60 offen offset:0 \n" \
  "s_add_u32 s60, s53, 3072\n" \
  "buffer_load_dwordx4 v[196:199], v1, s[40:43], s60 offen offset:128 \n" \
  "s_add_u32 s60, s53, 3072\n" \
  "buffer_load_dwordx4 v[200:203], v1, s[40:43], s60 offen offset:256 \n" \
  "s_add_u32 s60, s53, 3072\n" \
  "buffer_load_dwordx4 v[204:207], v1, s[40:43], s60 offen offset:384 \n" \
  "s_add_u32 s60, s53, 3072\n" \
  "buffer_load_dwordx4 v[208:211], v2, s[44:47], s60 offen offset:0 \n" \
  "s_add_u32 s60, s53, 39936\n" \
  "buffer_load_dwordx4 v[212:215], v2, s[44:47], s60 offen offset:0 \n" \
  "s_add_u32 s60, s53, 3072\n" \
  "buffer_load_dwordx4 v[216:219], v2, s[44:47], s60 offen offset:256 \n" \
  "s_add_u32 s60, s53, 39936\n" \
  "buffer_load_dwordx4 v[220:223], v2, s[44:47], s60 offen offset:256 \n" \
  "s_add_u32 m0, s92, 161792\n" \
  "s_add_u32 s60, s57, 32768\n" \
  "s_mov_b32 exec_hi, 0\n" \
  "buffer_load_dword v0, s[48:51], s60 offen sc1 lds\n" \
  "s_mov_b32 exec_hi, -1\n" \
  "s_mov_b32 s62, 0xaaaa\n" \
  "s_mov_b32 s63, 0x0\n" \
  "s_call_b64 s[66:67], LCW1_%=\n" \
  "s_nop 1\n" \
  "s_waitcnt vmcnt(1)\n" \
  "s_add_u32 s60, s53, 3584\n" \
  "buffer_load_dwordx4 v[224:227], v1, s[40:43], s60 offen offset:0 \n" \
  "s_add_u32 s60, s53, 3584\n" \
  "buffer_load_dwordx4 v[228:231], v1, s[40:43], s60 offen offset:128 \n" \
  "s_add_u32 s60, s53, 3584\n" \
  "buffer_load_dwordx4 v[232:235], v1, s[40:43], s60 offen offset:256 \n" \
  "s_add_u32 s60, s53, 3584\n" \
  "buffer_load_dwordx4 v[236:239], v1, s[40:43], s60 offen offset:384 \n" \
  "s_add_u32 s60, s53, 3584\n" \
  "buffer_load_dwordx4 v[240:243], v2, s[44:47], s60 offen offset:0 \n" \
  "s_add_u32 s60, s53, 40448\n" \
  "buffer_load_dwordx4 v[244:247], v2, s[44:47], s60 offen offset:0 \n" \
  "s_add_u32 s60, s53, 3584\n" \
  "buffer_load_dwordx4 v[248:251], v2, s[44:47], s60 offen offset:256 \n" \
  "s_add_u32 s60, s53, 40448\n" \
  "buffer_load_dwordx4 v[252:255], v2, s[44:47], s60 offen offset:256 \n" \
  "s_add_u32 m0, s92, 161792\n" \
  "s_add_u32 s60, s57, 32768\n" \
  "s_mov_b32 exec_hi, 0\n" \
  "buffer_load_dword v0, s[48:51], s60 offen sc1 lds\n" \
  "s_mov_b32 exec_hi, -1\n" \
  "s_mov_b32 s62, 0xaaaa\n" \
  "s_mov_b32 s63, 0x0\n" \
  "s_call_b64 s[66:67], LCW0_%=\n" \
  "s_nop 1\n" \
  "s_waitcnt vmcnt(1)\n" \
  "s_add_u32 s60, s53, 4096\n" \
  "buffer_load_dwordx4 v[192:195], v1, s[40:43], s60 offen offset:0 nt\n" \
  "s_add_u32 s60, s53, 4096\n" \
  "buffer_load_dwordx4 v[196:199], v1, s[40:43], s60 offen offset:128 nt\n" \
  "s_add_u32 s60, s53, 4096\n" \
  "buffer_load_dwordx4 v[200:203], v1, s[40:43], s60 offen offset:256 nt\n" \
  "s_add_u32 s60, s53, 4096\n" \
  "buffer_load_dwordx4 v[204:207], v1, s[40:43], s60 offen offset:384 nt\n" \
  "s_add_u32 s60, s53, 4096\n" \
  "buffer_load_dwordx4 v[208:211], v2, s[44:47], s60 offen offset:0 nt\n" \
  "s_add_u32 s60, s53, 40960\n" \
  "buffer_load_dwordx4 v[212:215], v2, s[44:47], s60 offen offset:0 nt\n" \
  "s_add_u32 s60, s53, 4096\n" \
  "buffer_load_dwordx4 v[216:219], v2, s[44:47], s60 offen offset:256 nt\n" \
  "s_add_u32 s60, s53, 40960\n" \
  "buffer_load_dwordx4 v[220:223], v2, s[44:47], s60 offen offset:256 nt\n" \
  "s_add_u32 m0, s92, 161792\n" \
  "s_add_u32 s60, s57, 32768\n" \
  "s_mov_b32 exec_hi, 0\n" \
  "buffer_load_dword v0, s[48:51], s60 offen sc1 lds\n" \
  "s_mov_b32 exec_hi, -1\n" \
  "s_mov_b32 s62, 0xaaaa\n" \
  "s_mov_b32 s63, 0x0\n" \
  "s_call_b64 s[66:67], LCW1_%=\n" \
  "s_nop 1\n" \
  "s_waitcnt vmcnt(1)\n" \
  "s_add_u32 s60, s53, 4608\n" \
  "buffer_load_dwordx4 v[224:227], v1, s[40:43], s60 offen offset:0 nt\n" \
  "s_add_u32 s60, s53, 4608\n" \
  "buffer_load_dwordx4 v[228:231], v1, s[40:43], s60 offen offset:128 nt\n" \
  "s_add_u32 s60, s53, 4608\n" \
  "buffer_load_dwordx4 v[232:235], v1, s[40:43], s60 offen offset:256 nt\n" \
  "s_add_u32 s60, s53, 4608\n" \
  "buffer_load_dwordx4 v[236:239], v1, s[40:43], s60 offen offset:384 nt\n" \
  "s_add_u32 s60, s53, 4608\n" \
  "buffer_load_dwordx4 v[240:243], v2, s[44:47], s60 offen offset:0 nt\n" \
  "s_add_u32 s60, s53, 41472\n" \
  "buffer_load_dwordx4 v[244:247], v2, s[44:47], s60 offen offset:0 nt\n" \
  "s_add_u32 s60, s53, 4608\n" \
  "buffer_load_dwordx4 v[248:251], v2, s[44:47], s60 offen offset:256 nt\n" \
  "s_add_u32 s60, s53, 41472\n" \
  "buffer_load_dwordx4 v[252:255], v2, s[44:47], s60 offen offset:256 nt\n" \
  "s_add_u32 m0, s92, 161792\n" \
  "s_add_u32 s60, s57, 32768\n" \
  "s_mov_b32 exec_hi, 0\n" \
  "buffer_load_dword v0, s[48:51], s60 offen sc1 lds\n" \
  "s_mov_b32 exec_hi, -1\n" \
  "s_cmp_lg_u32 %[grp], 0\n" \
  "s_cbranch_scc1 LrestB_%=\n" \
  "s_mov_b32 s70, 161792\n" \
  "s_add_u32 s71, s82, 32768\n" \
  "s_waitcnt lgkmcnt(0)\n" \
  "s_call_b64 s[66:67], LQL_%=\n" \
  "s_nop 1\n" \
  "s_memrealtime s[90:91]\n" \
  "s_waitcnt lgkmcnt(0)\n" \
  "s_mov_b32 s76, 0\n" \
  "s_mul_i32 s78, s56, 0xaaaa\n" \
  "s_mov_b32 s79, 0\n" \
  "s_waitcnt lgkmcnt(0)\n" \
  "s_call_b64 s[66:67], LB1_%=\n" \
  "s_nop 1\n" \
  "s_add_u32 s60, s53, 5120\n" \
  "buffer_load_dwordx4 a[64:67], v1, s[40:43], s60 offen offset:0 nt\n" \
  "s_add_u32 s60, s53, 5120\n" \
  "buffer_load_dwordx4 a[68:71], v1, s[40:43], s60 offen offset:128 nt\n" \
  "s_add_u32 s60, s53, 5120\n" \
  "buffer_load_dwordx4 a[72:75], v1, s[40:43], s60 offen offset:256 nt\n" \
  "s_add_u32 s60, s53, 5120\n" \
  "buffer_load_dwordx4 a[76:79], v1, s[40:43], s60 offen offset:384 nt\n" \
  "s_add_u32 s60, s53, 5632\n" \
  "buffer_load_dwordx4 a[96:99], v1, s[40:43], s60 offen offset:0 nt\n" \
  "s_add_u32 s60, s53, 5632\n" \
  "buffer_load_dwordx4 a[100:103], v1, s[40:43], s60 offen offset:128 nt\n" \
  "s_add_u32 s60, s53, 5632\n" \
  "buffer_load_dwordx4 a[104:107], v1, s[40:43], s60 offen offset:256 nt\n" \
  "s_add_u32 s60, s53, 5632\n" \
  "buffer_load_dwordx4 a[108:111], v1, s[40:43], s60 offen offset:384 nt\n" \
  "s_call_b64 s[66:67], LB3a_%=\n" \
  "s_nop 1\n" \
  "s_call_b64 s[66:67], LB3b_%=\n" \
  "s_nop 1\n" \
  "ds_write_b128 v3, v[24:27] offset:24576\n" \
  "ds_write_b128 v3, v[28:31] offset:25600\n" \
  "ds_write_b64 v3, v[6:7] offset:26624\n" \
  "s_waitcnt lgkmcnt(0)\n" \
  "v_mov_b64 v[24:25], v[160:161]\n" \
  "v_mov_b64 v[26:27], v[162:163]\n" \
  "v_mov_b64 v[28:29], v[164:165]\n" \
  "v_mov_b64 v[30:31], v[166:167]\n" \
  "v_mov_b32 v6, v168\n" \
  "v_mov_b32 v7, v169\n" \
  "v_mov_b64 v[160:161], 0\n" \
  "v_mov_b64 v[162:163], 0\n" \
  "v_mov_b64 v[164:165], 0\n" \
  "v_mov_b64 v[166:167], 0\n" \
  "v_mov_b64 v[168:169], 0\n" \
  "s_mov_b32 s70, 160768\n" \
  "s_add_u32 s71, s82, 16384\n" \
  "s_waitcnt lgkmcnt(0)\n" \
  "s_call_b64 s[66:67], LQL_%=\n" \
  "s_nop 1\n" \
  "s_mov_b32 s62, 0xaaaa\n" \
  "s_mov_b32 s63, 0x0\n" \
  "s_call_b64 s[66:67], LCW0_%=\n" \
  "s_nop 1\n" \
  "s_waitcnt vmcnt(13)\n" \
  "s_mov_b32 s62, 0xaaaa\n" \
  "s_mov_b32 s63, 0x0\n" \
  "s_call_b64 s[66:67], LCW1_%=\n" \
  "s_nop 1\n" \
  "s_add_u32 s60, s53, 5120\n" \
  "buffer_load_dwordx4 a[80:83], v2, s[44:47], s60 offen offset:0 nt\n" \
  "s_add_u32 s60, s53, 41984\n" \
  "buffer_load_dwordx4 a[84:87], v2, s[44:47], s60 offen offset:0 nt\n" \
  "s_add_u32 s60, s53, 5120\n" \
  "buffer_load_dwordx4 a[88:91], v2, s[44:47], s60 offen offset:256 nt\n" \
  "s_add_u32 s60, s53, 41984\n" \
  "buffer_load_dwordx4 a[92:95], v2, s[44:47], s60 offen offset:256 nt\n" \
  "s_add_u32 s60, s53, 5632\n" \
  "buffer_load_dwordx4 a[112:115], v2, s[44:47], s60 offen offset:0 nt\n" \
  "s_add_u32 s60, s53, 42496\n" \
  "buffer_load_dwordx4 a[116:119], v2, s[44:47], s60 offen offset:0 nt\n" \
  "s_add_u32 s60, s53, 5632\n" \
  "buffer_load_dwordx4 a[120:123], v2, s[44:47], s60 offen offset:256 nt\n" \
  "s_add_u32 s60, s53, 42496\n" \
  "buffer_load_dwordx4 a[124:127], v2, s[44:47], s60 offen offset:256 nt\n" \
  "s_cmp_eq_u32 s56, 0\n" \
  "s_cbranch_scc1 LM_5_%=\n" \
  "v_and_b32 v54, 0x38, v0\n" \
  "v_lshlrev_b32 v54, 7, v54\n" \
  "v_sub_u32 v54, v1, v54\n" \
  "s_add_u32 s60, s55, 0\n" \
  "s_add_u32 m0, s73, 0\n" \
  "s_nop 0\n" \
  "buffer_load_dwordx4 v54, s[40:43], s60 offen offset:0 nt lds\n" \
  "v_and_b32 v54, 0x38, v0\n" \
  "v_lshlrev_b32 v54, 7, v54\n" \
  "v_sub_u32 v54, v1, v54\n" \
  "s_add_u32 s60, s55, 0\n" \
  "s_add_u32 m0, s73, 896\n" \
  "s_nop 0\n" \
  "buffer_load_dwordx4 v54, s[40:43], s60 offen offset:128 nt lds\n" \
  "v_and_b32 v54, 0x38, v0\n" \
  "v_lshlrev_b32 v54, 7, v54\n" \
  "v_sub_u32 v54, v1, v54\n" \
  "s_add_u32 s60, s55, 0\n" \
  "s_add_u32 m0, s73, 1792\n" \
  "s_nop 0\n" \
  "buffer_load_dwordx4 v54, s[40:43], s60 offen offset:256 nt lds\n" \
  "v_and_b32 v54, 0x38, v0\n" \
  "v_lshlrev_b32 v54, 7, v54\n" \
  "v_sub_u32 v54, v1, v54\n" \
  "s_add_u32 s60, s55, 0\n" \
  "s_add_u32 m0, s73, 2688\n" \
  "s_nop 0\n" \
  "buffer_load_dwordx4 v54, s[40:43], s60 offen offset:384 nt lds\n" \
  "v_and_b32 v54, 0xc0, v0\n" \
  "v_lshlrev_b32 v54, 4, v54\n" \
  "v_sub_u32 v54, v2, v54\n" \
  "s_add_u32 s60, s55, 0\n" \
  "s_add_u32 m0, s73, 4096\n" \
  "s_nop 0\n" \
  "buffer_load_dwordx4 v54, s[44:47], s60 offen offset:0 nt lds\n" \
  "v_and_b32 v54, 0xc0, v0\n" \
  "v_lshlrev_b32 v54, 4, v54\n" \
  "v_sub_u32 v54, v2, v54\n" \
  "s_add_u32 s60, s55, 32768\n" \
  "s_add_u32 m0, s73, 5120\n" \
  "s_nop 0\n" \
  "buffer_load_dwordx4 v54, s[44:47], s60 offen offset:0 nt lds\n" \
  "v_and_b32 v54, 0xc0, v0\n" \
  "v_lshlrev_b32 v54, 4, v54\n" \
  "v_sub_u32 v54, v2, v54\n" \
  "s_add_u32 s60, s55, 0\n" \
  "s_add_u32 m0, s73, 5888\n" \
  "s_nop 0\n" \
  "buffer_load_dwordx4 v54, s[44:47], s60 offen offset:256 nt lds\n" \
  "v_and_b32 v54, 0xc0, v0\n" \
  "v_lshlrev_b32 v54, 4, v54\n" \
  "v_sub_u32 v54, v2, v54\n" \
  "s_add_u32 s60, s55, 32768\n" \
  "s_add_u32 m0, s73, 6912\n" \
  "s_nop 0\n" \
  "buffer_load_dwordx4 v54, s[44:47], s60 offen offset:256 nt lds\n" \
  "LM_5_%=:\n" \
  "s_add_u32 s60, s53, 6144\n" \
  "buffer_load_dwordx4 a[128:131], v1, s[40:43], s60 offen offset:0 nt\n" \
  "s_add_u32 s60, s53, 6144\n" \
  "buffer_load_dwordx4 a[132:135], v1, s[40:43], s60 offen offset:128 nt\n" \
  "s_add_u32 s60, s53, 6144\n" \
  "buffer_load_dwordx4 a[136:139], v1, s[40:43], s60 offen offset:256 nt\n" \
  "s_add_u32 s60, s53, 6144\n" \
  "buffer_load_dwordx4 a[140:143], v1, s[40:43], s60 offen offset:384 nt\n" \
  "s_add_u32 s60, s53, 6144\n" \
  "buffer_load_dwordx4 a[144:147], v2, s[44:47], s60 offen offset:0 nt\n" \
  "s_add_u32 s60, s53, 43008\n" \
  "buffer_load_dwordx4 a[148:151], v2, s[44:47], s60 offen offset:0 nt\n" \
  "s_add_u32 s60, s53, 6144\n" \
  "buffer_load_dwordx4 a[152:155], v2, s[44:47], s60 offen offset:256 nt\n" \
  "s_add_u32 s60, s53, 43008\n" \
  "buffer_load_dwordx4 a[156:159], v2, s[44:47], s60 offen offset:256 nt\n" \
  "s_add_u32 s60, s53, 6656\n" \
  "buffer_load_dwordx4 a[160:163], v1, s[40:43], s60 offen offset:0 nt\n" \
  "s_add_u32 s60, s53, 6656\n" \
  "buffer_load_dwordx4 a[164:167], v1, s[40:43], s60 offen offset:128 nt\n" \
  "s_add_u32 s60, s53, 6656\n" \
  "buffer_load_dwordx4 a[168:171], v1, s[40:43], s60 offen offset:256 nt\n" \
  "s_add_u32 s60, s53, 6656\n" \
  "buffer_load_dwordx4 a[172:175], v1, s[40:43], s60 offen offset:384 nt\n" \
  "s_add_u32 s60, s53, 6656\n" \
  "buffer_load_dwordx4 a[176:179], v2, s[44:47], s60 offen offset:0 nt\n" \
  "s_add_u32 s60, s53, 43520\n" \
  "buffer_load_dwordx4 a[180:183], v2, s[44:47], s60 offen offset:0 nt\n" \
  "s_add_u32 s60, s53, 6656\n" \
  "buffer_load_dwordx4 a[184:187], v2, s[44:47], s60 offen offset:256 nt\n" \
  "s_add_u32 s60, s53, 43520\n" \
  "buffer_load_dwordx4 a[188:191], v2, s[44:47], s60 offen offset:256 nt\n" \
  "s_add_u32 s60, s53, 7168\n" \
  "buffer_load_dwordx4 a[192:195], v1, s[40:43], s60 offen offset:0 nt\n" \
  "s_add_u32 s60, s53, 7168\n" \
  "buffer_load_dwordx4 a[196:199], v1, s[40:43], s60 offen offset:128 nt\n" \
  "s_add_u32 s60, s53, 7168\n" \
  "buffer_load_dwordx4 a[200:203], v1, s[40:43], s60 offen offset:256 nt\n" \
  "s_add_u32 s60, s53, 7168\n" \
  "buffer_load_dwordx4 a[204:207], v1, s[40:43], s60 offen offset:384 nt\n" \
  "s_add_u32 s60, s53, 7168\n" \
  "buffer_load_dwordx4 a[208:211], v2, s[44:47], s60 offen offset:0 nt\n" \
  "s_add_u32 s60, s53, 44032\n" \
  "buffer_load_dwordx4 a[212:215], v2, s[44:47], s60 offen offset:0 nt\n" \
  "s_add_u32 s60, s53, 7168\n" \
  "buffer_load_dwordx4 a[216:219], v2, s[44:47], s60 offen offset:256 nt\n" \
  "s_add_u32 s60, s53, 44032\n" \
  "buffer_load_dwordx4 a[220:223], v2, s[44:47], s60 offen offset:256 nt\n" \
  "s_waitcnt vmcnt(47)\n" \
  "s_add_u32 s60, s53, 7680\n" \
  "buffer_load_dwordx4 a[240:243], v2, s[44:47], s60 offen offset:0 nt\n" \
  "s_add_u32 s60, s53, 44544\n" \
  "buffer_load_dwordx4 a[244:247], v2, s[44:47], s60 offen offset:0 nt\n" \
  "s_add_u32 s60, s53, 7680\n" \
  "buffer_load_dwordx4 a[248:251], v2, s[44:47], s60 offen offset:256 nt\n" \
  "s_add_u32 s60, s53, 44544\n" \
  "buffer_load_dwordx4 a[252:255], v2, s[44:47], s60 offen offset:256 nt\n" \
  "s_waitcnt vmcnt(47)\n" \
  "s_add_u32 s60, s53, 8192\n" \
  "buffer_load_dwordx4 v[64:67], v1, s[40:43], s60 offen offset:0 nt\n" \
  "s_add_u32 s60, s53, 8192\n" \
  "buffer_load_dwordx4 v[68:71], v1, s[40:43], s60 offen offset:128 nt\n" \
  "s_add_u32 s60, s53, 8192\n" \
  "buffer_load_dwordx4 v[72:75], v1, s[40:43], s60 offen offset:256 nt\n" \
  "s_add_u32 s60, s53, 8192\n" \
  "buffer_load_dwordx4 v[76:79], v1, s[40:43], s60 offen offset:384 nt\n" \
  "s_add_u32 s60, s53, 8192\n" \
  "buffer_load_dwordx4 v[80:83], v2, s[44:47], s60 offen offset:0 nt\n" \
  "s_add_u32 s60, s53, 45056\n" \
  "buffer_load_dwordx4 v[84:87], v2, s[44:47], s60 offen offset:0 nt\n" \
  "s_add_u32 s60, s53, 8192\n" \
  "buffer_load_dwordx4 v[88:91], v2, s[44:47], s60 offen offset:256 nt\n" \
  "s_add_u32 s60, s53, 45056\n" \
  "buffer_load_dwordx4 v[92:95], v2, s[44:47], s60 offen offset:256 nt\n" \
  "s_waitcnt vmcnt(47)\n" \
  "s_add_u32 s60, s53, 8704\n" \
  "buffer_load_dwordx4 v[96:99], v1, s[40:43], s60 offen offset:0 nt\n" \
  "s_add_u32 s60, s53, 8704\n" \
  "buffer_load_dwordx4 v[100:103], v1, s[40:43], s60 offen offset:128 nt\n" \
  "s_add_u32 s60, s53, 8704\n" \
  "buffer_load_dwordx4 v[104:107], v1, s[40:43], s60 offen offset:256 nt\n" \
  "s_add_u32 s60, s53, 8704\n" \
  "buffer_load_dwordx4 v[108:111], v1, s[40:43], s60 offen offset:384 nt\n" \
  "s_add_u32 s60, s53, 8704\n" \
  "buffer_load_dwordx4 v[112:115], v2, s[44:47], s60 offen offset:0 nt\n" \
  "s_add_u32 s60, s53, 45568\n" \
  "buffer_load_dwordx4 v[116:119], v2, s[44:47], s60 offen offset:0 nt\n" \
  "s_add_u32 s60, s53, 8704\n" \
  "buffer_load_dwordx4 v[120:123], v2, s[44:47], s60 offen offset:256 nt\n" \
  "s_add_u32 s60, s53, 45568\n" \
  "buffer_load_dwordx4 v[124:127], v2, s[44:47], s60 offen offset:256 nt\n" \
  "s_waitcnt vmcnt(48)\n" \
  "s_waitcnt vmcnt(44)\n" \
  "s_cmp_eq_u32 s56, 0\n" \
  "s_cbranch_scc1 LM_6_%=\n" \
  "s_waitcnt vmcnt(44)\n" \
  "v_lshlrev_b32 v55, 2, v0\n" \
  "v_add_u32 v55, s73, v55\n" \
  "ds_read_b128 a[0:3], v55 offset:0\n" \
  "ds_read_b128 a[4:7], v55 offset:1024\n" \
  "ds_read_b128 a[8:11], v55 offset:2048\n" \
  "ds_read_b128 a[12:15], v55 offset:3072\n" \
  "ds_read_b128 a[16:19], v55 offset:4096\n" \
  "ds_read_b128 a[20:23], v55 offset:5120\n" \
  "ds_read_b128 a[24:27], v55 offset:6144\n" \
  "ds_read_b128 a[28:31], v55 offset:7168\n" \
  "s_waitcnt lgkmcnt(0)\n" \
  "LM_6_%=:\n" \
  "s_mul_i32 s62, s56, 0xaaaa\n" \
  "s_mov_b32 s63, 0xaaaaaaaa\n" \
  "s_call_b64 s[66:67], LC40_%=\n" \
  "s_nop 1\n" \
  "s_waitcnt vmcnt(36)\n" \
  "s_waitcnt vmcnt(28)\n" \
  "s_waitcnt vmcnt(20)\n" \
  "s_waitcnt vmcnt(16)\n" \
  "s_mov_b32 s62, 0xaaaaaaaa\n" \
  "s_mov_b32 s63, 0xaaaaaaaa\n" \
  "s_call_b64 s[66:67], LC41_%=\n" \
  "s_nop 1\n" \
  "s_waitcnt vmcnt(8)\n" \
  "s_waitcnt vmcnt(0)\n" \
  "s_mov_b32 s62, 0xaaaaaaaa\n" \
  "s_mov_b32 s63, 0x0\n" \
  "s_call_b64 s[66:67], LC42_%=\n" \
  "s_nop 1\n" \
  "s_add_u32 s70, s58, 243712\n" \
  "s_mov_b32 s71, 151552\n" \
  "s_mov_b32 s77, 155680\n" \
  "s_waitcnt lgkmcnt(0)\n" \
  "s_call_b64 s[66:67], LDU_%=\n" \
  "s_nop 1\n" \
  "s_memrealtime s[90:91]\n" \
  "s_add_u32 s60, s52, 8192\n" \
  "buffer_load_dwordx4 a[0:3], v1, s[40:43], s60 offen offset:0 nt\n" \
  "s_add_u32 s60, s52, 8192\n" \
  "buffer_load_dwordx4 a[4:7], v1, s[40:43], s60 offen offset:128 nt\n" \
  "s_add_u32 s60, s52, 8192\n" \
  "buffer_load_dwordx4 a[8:11], v1, s[40:43], s60 offen offset:256 nt\n" \
  "s_add_u32 s60, s52, 8192\n" \
  "buffer_load_dwordx4 a[12:15], v1, s[40:43], s60 offen offset:384 nt\n" \
  "s_add_u32 s60, s52, 8192\n" \
  "buffer_load_dwordx4 a[16:19], v2, s[44:47], s60 offen offset:0 nt\n" \
  "s_add_u32 s60, s52, 45056\n" \
  "buffer_load_dwordx4 a[20:23], v2, s[44:47], s60 offen offset:0 nt\n" \
  "s_add_u32 s60, s52, 8192\n" \
  "buffer_load_dwordx4 a[24:27], v2, s[44:47], s60 offen offset:256 nt\n" \
  "s_add_u32 s60, s52, 45056\n" \
  "buffer_load_dwordx4 a[28:31], v2, s[44:47], s60 offen offset:256 nt\n" \
  "s_add_u32 s60, s52, 8704\n" \
  "buffer_load_dwordx4 a[32:35], v1, s[40:43], s60 offen offset:0 nt\n" \
  "s_add_u32 s60, s52, 8704\n" \
  "buffer_load_dwordx4 a[36:39], v1, s[40:43], s60 offen offset:128 nt\n" \
  "s_add_u32 s60, s52, 8704\n" \
  "buffer_load_dwordx4 a[40:43], v1, s[40:43], s60 offen offset:256 nt\n" \
  "s_add_u32 s60, s52, 8704\n" \
  "buffer_load_dwordx4 a[44:47], v1, s[40:43], s60 offen offset:384 nt\n" \
  "s_add_u32 s60, s52, 8704\n" \
  "buffer_load_dwordx4 a[48:51], v2, s[44:47], s60 offen offset:0 nt\n" \
  "s_add_u32 s60, s52, 45568\n" \
  "buffer_load_dwordx4 a[52:55], v2, s[44:47], s60 offen offset:0 nt\n" \
  "s_add_u32 s60, s52, 8704\n" \
  "buffer_load_dwordx4 a[56:59], v2, s[44:47], s60 offen offset:256 nt\n" \
  "s_add_u32 s60, s52, 45568\n" \
  "buffer_load_dwordx4 a[60:63], v2, s[44:47], s60 offen offset:256 nt\n" \
  "s_add_u32 s60, s53, 0\n" \
  "buffer_load_dwordx4 v[128:131], v1, s[40:43], s60 offen offset:0 nt\n" \
  "s_add_u32 s60, s53, 0\n" \
  "buffer_load_dwordx4 v[132:135], v1, s[40:43], s60 offen offset:128 nt\n" \
  "s_add_u32 s60, s53, 0\n" \
  "buffer_load_dwordx4 v[136:139], v1, s[40:43], s60 offen offset:256 nt\n" \
  "s_add_u32 s60, s53, 0\n" \
  "buffer_load_dwordx4 v[140:143], v1, s[40:43], s60 offen offset:384 nt\n" \
  "s_add_u32 s60, s53, 0\n" \
  "buffer_load_dwordx4 v[144:147], v2, s[44:47], s60 offen offset:0 nt\n" \
  "s_add_u32 s60, s53, 36864\n" \
  "buffer_load_dwordx4 v[148:151], v2, s[44:47], s60 offen offset:0 nt\n" \
  "s_add_u32 s60, s53, 0\n" \
  "buffer_load_dwordx4 v[152:155], v2, s[44:47], s60 offen offset:256 nt\n" \
  "s_add_u32 s60, s53, 36864\n" \
  "buffer_load_dwordx4 v[156:159], v2, s[44:47], s60 offen offset:256 nt\n" \
  "ds_read_b128 v[24:27], v3 offset:24576\n" \
  "ds_read_b128 v[28:31], v3 offset:25600\n" \
  "ds_read_b64 v[6:7], v3 offset:26624\n" \
  "s_waitcnt lgkmcnt(0)\n" \
  "s_mov_b32 s70, 161792\n" \
  "s_add_u32 s71, s82, 32768\n" \
  "s_waitcnt lgkmcnt(0)\n" \
  "s_call_b64 s[66:67], LQL_%=\n" \
  "s_nop 1\n" \
  "s_waitcnt vmcnt(8)\n" \
  "s_mov_b32 s62, 0xaaaaaaaa\n" \
  "s_mov_b32 s63, 0x0\n" \
  "s_call_b64 s[66:67], LC40_%=\n" \
  "s_nop 1\n" \
  "s_add_u32 s70, s58, 487424\n" \
  "s_mov_b32 s71, 147456\n" \
  "s_mov_b32 s77, 155648\n" \
  "s_waitcnt lgkmcnt(0)\n" \
  "s_call_b64 s[66:67], LDU_%=\n" \
  "s_nop 1\n" \
  "s_add_u32 s60, s53, 3072\n" \
  "s_add_u32 m0, s69, 0\n" \
  "s_nop 0\n" \
  "buffer_load_dwordx4 v1, s[40:43], s60 offen offset:0 nt lds\n" \
  "s_add_u32 s60, s53, 3072\n" \
  "s_add_u32 m0, s69, 896\n" \
  "s_nop 0\n" \
  "buffer_load_dwordx4 v1, s[40:43], s60 offen offset:128 nt lds\n" \
  "s_add_u32 s60, s53, 3072\n" \
  "s_add_u32 m0, s69, 1792\n" \
  "s_nop 0\n" \
  "buffer_load_dwordx4 v1, s[40:43], s60 offen offset:256 nt lds\n" \
  "s_add_u32 s60, s53, 3072\n" \
  "s_add_u32 m0, s69, 2688\n" \
  "s_nop 0\n" \
  "buffer_load_dwordx4 v1, s[40:43], s60 offen offset:384 nt lds\n" \
  "s_add_u32 s60, s53, 3072\n" \
  "s_add_u32 m0, s69, 4096\n" \
  "s_nop 0\n" \
  "buffer_load_dwordx4 v2, s[44:47], s60 offen offset:0 nt lds\n" \
  "s_add_u32 s60, s53, 39936\n" \
  "s_add_u32 m0, s69, 5120\n" \
  "s_nop 0\n" \
  "buffer_load_dwordx4 v2, s[44:47], s60 offen offset:0 nt lds\n" \
  "s_add_u32 s60, s53, 3072\n" \
  "s_add_u32 m0, s69, 5888\n" \
  "s_nop 0\n" \
  "buffer_load_dwordx4 v2, s[44:47], s60 offen offset:256 nt lds\n" \
  "s_add_u32 s60, s53, 39936\n" \
  "s_add_u32 m0, s69, 6912\n" \
  "s_nop 0\n" \
  "buffer_load_dwordx4 v2, s[44:47], s60 offen offset:256 nt lds\n" \
  "s_add_u32 s60, s53, 3584\n" \
  "s_add_u32 m0, s69, 8192\n" \
  "s_nop 0\n" \
  "buffer_load_dwordx4 v1, s[40:43], s60 offen offset:0 nt lds\n" \
  "s_add_u32 s60, s53, 3584\n" \
  "s_add_u32 m0, s69, 9088\n" \
  "s_nop 0\n" \
  "buffer_load_dwordx4 v1, s[40:43], s60 offen offset:128 nt lds\n" \
  "s_add_u32 s60, s53, 3584\n" \
  "s_add_u32 m0, s69, 9984\n" \
  "s_nop 0\n" \
  "buffer_load_dwordx4 v1, s[40:43], s60 offen offset:256 nt lds\n" \
  "s_add_u32 s60, s53, 3584\n" \
  "s_add_u32 m0, s69, 10880\n" \
  "s_nop 0\n" \
  "buffer_load_dwordx4 v1, s[40:43], s60 offen offset:384 nt lds\n" \
  "s_add_u32 s60, s53, 3584\n" \
  "s_add_u32 m0, s69, 12288\n" \
  "s_nop 0\n" \
  "buffer_load_dwordx4 v2, s[44:47], s60 offen offset:0 nt lds\n" \
  "s_add_u32 s60, s53, 40448\n" \
  "s_add_u32 m0, s69, 13312\n" \
  "s_nop 0\n" \
  "buffer_load_dwordx4 v2, s[44:47], s60 offen offset:0 nt lds\n" \
  "s_add_u32 s60, s53, 3584\n" \
  "s_add_u32 m0, s69, 14080\n" \
  "s_nop 0\n" \
  "buffer_load_dwordx4 v2, s[44:47], s60 offen offset:256 nt lds\n" \
  "s_add_u32 s60, s53, 40448\n" \
  "s_add_u32 m0, s69, 15104\n" \
  "s_nop 0\n" \
  "buffer_load_dwordx4 v2, s[44:47], s60 offen offset:256 nt lds\n" \
  "LM_7_%=:\n" \
  "s_memrealtime s[84:85]\n" \
  "s_waitcnt lgkmcnt(0)\n" \
  "s_sub_u32 s84, s84, s90\n" \
  "s_cmp_ge_u32 s84, 100\n" \
  "s_cbranch_scc1 LM_8_%=\n" \
  "s_sleep 1\n" \
  "s_branch LM_7_%=\n" \
  "LM_8_%=:\n" \
  "s_add_u32 s60, s53, 512\n" \
  "buffer_load_dwordx4 v[160:163], v1, s[40:43], s60 offen offset:0 nt\n" \
  "s_add_u32 s60, s53, 512\n" \
  "buffer_load_dwordx4 v[164:167], v1, s[40:43], s60 offen offset:128 nt\n" \
  "s_add_u32 s60, s53, 512\n" \
  "buffer_load_dwordx4 v[168:171], v1, s[40:43], s60 offen offset:256 nt\n" \
  "s_add_u32 s60, s53, 512\n" \
  "buffer_load_dwordx4 v[172:175], v1, s[40:43], s60 offen offset:384 nt\n" \
  "s_add_u32 s60, s53, 512\n" \
  "buffer_load_dwordx4 v[176:179], v2, s[44:47], s60 offen offset:0 nt\n" \
  "s_add_u32 s60, s53, 37376\n" \
  "buffer_load_dwordx4 v[180:183], v2, s[44:47], s60 offen offset:0 nt\n" \
  "s_add_u32 s60, s53, 512\n" \
  "buffer_load_dwordx4 v[184:187], v2, s[44:47], s60 offen offset:256 nt\n" \
  "s_add_u32 s60, s53, 37376\n" \
  "buffer_load_dwordx4 v[188:191], v2, s[44:47], s60 offen offset:256 nt\n" \
  "s_add_u32 m0, s92, 162816\n" \
  "s_add_u32 s60, s57, 49152\n" \
  "s_mov_b32 exec_hi, 0\n" \
  "buffer_load_dword v0, s[48:51], s60 offen sc1 lds\n" \
  "s_mov_b32 exec_hi, -1\n" \
  "LM_9_%=:\n" \
  "s_memrealtime s[84:85]\n" \
  "s_waitcnt lgkmcnt(0)\n" \
  "s_sub_u32 s84, s84, s90\n" \
  "s_cmp_ge_u32 s84, 200\n" \
  "s_cbranch_scc1 LM_10_%=\n" \
  "s_sleep 1\n" \
  "s_branch LM_9_%=\n" \
  "LM_10_%=:\n" \
  "s_add_u32 s60, s53, 1024\n" \
  "s_add_u32 m0, s69, 16384\n" \
  "s_nop 0\n" \
  "buffer_load_dwordx4 v1, s[40:43], s60 offen offset:0 nt lds\n" \
  "s_add_u32 s60, s53, 1024\n" \
  "s_add_u32 m0, s69, 17280\n" \
  "s_nop 0\n" \
  "buffer_load_dwordx4 v1, s[40:43], s60 offen offset:128 nt lds\n" \
  "s_add_u32 s60, s53, 1024\n" \
  "s_add_u32 m0, s69, 18176\n" \
  "s_nop 0\n" \
  "buffer_load_dwordx4 v1, s[40:43], s60 offen offset:256 nt lds\n" \
  "s_add_u32 s60, s53, 1024\n" \
  "s_add_u32 m0, s69, 19072\n" \
  "s_nop 0\n" \
  "buffer_load_dwordx4 v1, s[40:43], s60 offen offset:384 nt lds\n" \
  "s_add_u32 s60, s53, 1024\n" \
  "s_add_u32 m0, s69, 20480\n" \
  "s_nop 0\n" \
  "buffer_load_dwordx4 v2, s[44:47], s60 offen offset:0 nt lds\n" \
  "s_add_u32 s60, s53, 37888\n" \
  "s_add_u32 m0, s69, 21504\n" \
  "s_nop 0\n" \
  "buffer_load_dwordx4 v2, s[44:47], s60 offen offset:0 nt lds\n" \
  "s_add_u32 s60, s53, 1024\n" \
  "s_add_u32 m0, s69, 22272\n" \
  "s_nop 0\n" \
  "buffer_load_dwordx4 v2, s[44:47], s60 offen offset:256 nt lds\n" \
  "s_add_u32 s60, s53, 37888\n" \
  "s_add_u32 m0, s69, 23296\n" \
  "s_nop 0\n" \
  "buffer_load_dwordx4 v2, s[44:47], s60 offen offset:256 nt lds\n" \
  "s_add_u32 m0, s92, 162816\n" \
  "s_add_u32 s60, s57, 49152\n" \
  "s_mov_b32 exec_hi, 0\n" \
  "buffer_load_dword v0, s[48:51], s60 offen sc1 lds\n" \
  "s_mov_b32 exec_hi, -1\n" \
  "LM_11_%=:\n" \
  "s_memrealtime s[84:85]\n" \
  "s_waitcnt lgkmcnt(0)\n" \
  "s_sub_u32 s84, s84, s90\n" \
  "s_cmp_ge_u32 s84, 300\n" \
  "s_cbranch_scc1 LM_12_%=\n" \
  "s_sleep 1\n" \
  "s_branch LM_11_%=\n" \
  "LM_12_%=:\n" \
  "s_add_u32 s60, s53, 1536\n" \
  "s_add_u32 m0, s69, 24576\n" \
  "s_nop 0\n" \
  "buffer_load_dwordx4 v1, s[40:43], s60 offen offset:0 nt lds\n" \
  "s_add_u32 s60, s53, 1536\n" \
  "s_add_u32 m0, s69, 25472\n" \
  "s_nop 0\n" \
  "buffer_load_dwordx4 v1, s[40:43], s60 offen offset:128 nt lds\n" \
  "s_add_u32 s60, s53, 1536\n" \
  "s_add_u32 m0, s69, 26368\n" \
  "s_nop 0\n" \
  "buffer_load_dwordx4 v1, s[40:43], s60 offen offset:256 nt lds\n" \
  "s_add_u32 s60, s53, 1536\n" \
  "s_add_u32 m0, s69, 27264\n" \
  "s_nop 0\n" \
  "buffer_load_dwordx4 v1, s[40:43], s60 offen offset:384 nt lds\n" \
  "s_add_u32 s60, s53, 1536\n" \
  "s_add_u32 m0, s69, 28672\n" \
  "s_nop 0\n" \
  "buffer_load_dwordx4 v2, s[44:47], s60 offen offset:0 nt lds\n" \
  "s_add_u32 s60, s53, 38400\n" \
  "s_add_u32 m0, s69, 29696\n" \
  "s_nop 0\n" \
  "buffer_load_dwordx4 v2, s[44:47], s60 offen offset:0 nt lds\n" \
  "s_add_u32 s60, s53, 1536\n" \
  "s_add_u32 m0, s69, 30464\n" \
  "s_nop 0\n" \
  "buffer_load_dwordx4 v2, s[44:47], s60 offen offset:256 nt lds\n" \
  "s_add_u32 s60, s53, 38400\n" \
  "s_add_u32 m0, s69, 31488\n" \
  "s_nop 0\n" \
  "buffer_load_dwordx4 v2, s[44:47], s60 offen offset:256 nt lds\n" \
  "s_add_u32 m0, s92, 162816\n" \
  "s_add_u32 s60, s57, 49152\n" \
  "s_mov_b32 exec_hi, 0\n" \
  "buffer_load_dword v0, s[48:51], s60 offen sc1 lds\n" \
  "s_mov_b32 exec_hi, -1\n" \
  "LM_13_%=:\n" \
  "s_memrealtime s[84:85]\n" \
  "s_waitcnt lgkmcnt(0)\n" \
  "s_sub_u32 s84, s84, s90\n" \
  "s_cmp_ge_u32 s84, 400\n" \
  "s_cbranch_scc1 LM_14_%=\n" \
  "s_sleep 1\n" \
  "s_branch LM_13_%=\n" \
  "LM_14_%=:\n" \
  "s_add_u32 s60, s53, 2048\n" \
  "buffer_load_dwordx4 a[32:35], v1, s[40:43], s60 offen offset:0 nt\n" \
  "s_add_u32 s60, s53, 2048\n" \
  "buffer_load_dwordx4 a[36:39], v1, s[40:43], s60 offen offset:128 nt\n" \
  "s_add_u32 s60, s53, 2048\n" \
  "buffer_load_dwordx4 a[40:43], v1, s[40:43], s60 offen offset:256 nt\n" \
  "s_add_u32 s60, s53, 2048\n" \
  "buffer_load_dwordx4 a[44:47], v1, s[40:43], s60 offen offset:384 nt\n" \
  "s_add_u32 s60, s53, 2048\n" \
  "buffer_load_dwordx4 a[48:51], v2, s[44:47], s60 offen offset:0 nt\n" \
  "s_add_u32 s60, s53, 38912\n" \
  "buffer_load_dwordx4 a[52:55], v2, s[44:47], s60 offen offset:0 nt\n" \
  "s_add_u32 s60, s53, 2048\n" \
  "buffer_load_dwordx4 a[56:59], v2, s[44:47], s60 offen offset:256 nt\n" \
  "s_add_u32 s60, s53, 38912\n" \
  "buffer_load_dwordx4 a[60:63], v2, s[44:47], s60 offen offset:256 nt\n" \
  "s_add_u32 m0, s92, 162816\n" \
  "s_add_u32 s60, s57, 49152\n" \
  "s_mov_b32 exec_hi, 0\n" \
  "buffer_load_dword v0, s[48:51], s60 offen sc1 lds\n" \
  "s_mov_b32 exec_hi, -1\n" \
  "LM_15_%=:\n" \
  "s_memrealtime s[84:85]\n" \
  "s_waitcnt lgkmcnt(0)\n" \
  "s_sub_u32 s84, s84, s90\n" \
  "s_cmp_ge_u32 s84, 500\n" \
  "s_cbranch_scc1 LM_16_%=\n" \
  "s_sleep 1\n" \
  "s_branch LM_15_%=\n" \
  "LM_16_%=:\n" \
  "s_waitcnt vmcnt(55)\n" \
  "s_add_u32 s60, s53, 2560\n" \
  "buffer_load_dwordx4 a[0:3], v1, s[40:43], s60 offen offset:0 nt\n" \
  "s_add_u32 s60, s53, 2560\n" \
  "buffer_load_dwordx4 a[4:7], v1, s[40:43], s60 offen offset:128 nt\n" \
  "s_add_u32 s60, s53, 2560\n" \
  "buffer_load_dwordx4 a[8:11], v1, s[40:43], s60 offen offset:256 nt\n" \
  "s_add_u32 s60, s53, 2560\n" \
  "buffer_load_dwordx4 a[12:15], v1, s[40:43], s60 offen offset:384 nt\n" \
  "s_add_u32 s60, s53, 2560\n" \
  "buffer_load_dwordx4 a[16:19], v2, s[44:47], s60 offen offset:0 nt\n" \
  "s_add_u32 s60, s53, 39424\n" \
  "buffer_load_dwordx4 a[20:23], v2, s[44:47], s60 offen offset:0 nt\n" \
  "s_add_u32 s60, s53, 2560\n" \
  "buffer_load_dwordx4 a[24:27], v2, s[44:47], s60 offen offset:256 nt\n" \
  "s_add_u32 s60, s53, 39424\n" \
  "buffer_load_dwordx4 a[28:31], v2, s[44:47], s60 offen offset:256 nt\n" \
  "s_add_u32 m0, s92, 162816\n" \
  "s_add_u32 s60, s57, 49152\n" \
  "s_mov_b32 exec_hi, 0\n" \
  "buffer_load_dword v0, s[48:51], s60 offen sc1 lds\n" \
  "s_mov_b32 exec_hi, -1\n" \
  "s_branch Lph6_%=\n" \
  "LrestB_%=:\n" \
  "s_nop 7\n" \
  "s_mov_b32 s70, 161792\n" \
  "s_add_u32 s71, s82, 32768\n" \
  "s_waitcnt lgkmcnt(0)\n" \
  "s_call_b64 s[66:67], LQL_%=\n" \
  "s_nop 1\n" \
  "s_memrealtime s[90:91]\n" \
  "s_waitcnt lgkmcnt(0)\n" \
  "s_mov_b32 s76, 0\n" \
  "s_mul_i32 s78, s56, 0xaaaa\n" \
  "s_mov_b32 s79, 0\n" \
  "s_waitcnt lgkmcnt(0)\n" \
  "s_call_b64 s[66:67], LB1_%=\n" \
  "s_nop 1\n" \
  "s_add_u32 s60, s53, 5120\n" \
  "buffer_load_dwordx4 a[64:67], v1, s[40:43], s60 offen offset:0 nt\n" \
  "s_add_u32 s60, s53, 5120\n" \
  "buffer_load_dwordx4 a[68:71], v1, s[40:43], s60 offen offset:128 nt\n" \
  "s_add_u32 s60, s53, 5120\n" \
  "buffer_load_dwordx4 a[72:75], v1, s[40:43], s60 offen offset:256 nt\n" \
  "s_add_u32 s60, s53, 5120\n" \
  "buffer_load_dwordx4 a[76:79], v1, s[40:43], s60 offen offset:384 nt\n" \
  "s_add_u32 s60, s53, 5632\n" \
  "buffer_load_dwordx4 a[96:99], v1, s[40:43], s60 offen offset:0 nt\n" \
  "s_add_u32 s60, s53, 5632\n" \
  "buffer_load_dwordx4 a[100:103], v1, s[40:43], s60 offen offset:128 nt\n" \
  "s_add_u32 s60, s53, 5632\n" \
  "buffer_load_dwordx4 a[104:107], v1, s[40:43], s60 offen offset:256 nt\n" \
  "s_add_u32 s60, s53, 5632\n" \
  "buffer_load_dwordx4 a[108:111], v1, s[40:43], s60 offen offset:384 nt\n" \
  "s_call_b64 s[66:67], LB3a_%=\n" \
  "s_nop 1\n" \
  "s_call_b64 s[66:67], LB3b_%=\n" \
  "s_nop 1\n" \
  "ds_write_b128 v3, v[24:27] offset:24576\n" \
  "ds_write_b128 v3, v[28:31] offset:25600\n" \
  "ds_write_b64 v3, v[6:7] offset:26624\n" \
  "s_waitcnt lgkmcnt(0)\n" \
  "v_mov_b64 v[24:25], v[160:161]\n" \
  "v_mov_b64 v[26:27], v[162:163]\n" \
  "v_mov_b64 v[28:29], v[164:165]\n" \
  "v_mov_b64 v[30:31], v[166:167]\n" \
  "v_mov_b32 v6, v168\n" \
  "v_mov_b32 v7, v169\n" \
  "v_mov_b64 v[160:161], 0\n" \
  "v_mov_b64 v[162:163], 0\n" \
  "v_mov_b64 v[164:165], 0\n" \
  "v_mov_b64 v[166:167], 0\n" \
  "v_mov_b64 v[168:169], 0\n" \
  "s_mov_b32 s70, 160768\n" \
  "s_add_u32 s71, s82, 16384\n" \
  "s_waitcnt lgkmcnt(0)\n" \
  "s_call_b64 s[66:67], LQL_%=\n" \
  "s_nop 1\n" \
  "s_mov_b32 s62, 0xaaaa\n" \
  "s_mov_b32 s63, 0x0\n" \
  "s_call_b64 s[66:67], LCW0_%=\n" \
  "s_nop 1\n" \
  "s_waitcnt vmcnt(13)\n" \
  "s_mov_b32 s62, 0xaaaa\n" \
  "s_mov_b32 s63, 0x0\n" \
  "s_call_b64 s[66:67], LCW1_%=\n" \
  "s_nop 1\n" \
  "s_add_u32 s60, s53, 5120\n" \
  "buffer_load_dwordx4 a[80:83], v2, s[44:47], s60 offen offset:0 nt\n" \
  "s_add_u32 s60, s53, 41984\n" \
  "buffer_load_dwordx4 a[84:87], v2, s[44:47], s60 offen offset:0 nt\n" \
  "s_add_u32 s60, s53, 5120\n" \
  "buffer_load_dwordx4 a[88:91], v2, s[44:47], s60 offen offset:256 nt\n" \
  "s_add_u32 s60, s53, 41984\n" \
  "buffer_load_dwordx4 a[92:95], v2, s[44:47], s60 offen offset:256 nt\n" \
  "s_add_u32 s60, s53, 5632\n" \
  "buffer_load_dwordx4 a[112:115], v2, s[44:47], s60 offen offset:0 nt\n" \
  "s_add_u32 s60, s53, 42496\n" \
  "buffer_load_dwordx4 a[116:119], v2, s[44:47], s60 offen offset:0 nt\n" \
  "s_add_u32 s60, s53, 5632\n" \
  "buffer_load_dwordx4 a[120:123], v2, s[44:47], s60 offen offset:256 nt\n" \
  "s_add_u32 s60, s53, 42496\n" \
  "buffer_load_dwordx4 a[124:127], v2, s[44:47], s60 offen offset:256 nt\n" \
  "s_cmp_eq_u32 s56, 0\n" \
  "s_cbranch_scc1 LM_17_%=\n" \
  "v_and_b32 v54, 0x38, v0\n" \
  "v_lshlrev_b32 v54, 7, v54\n" \
  "v_sub_u32 v54, v1, v54\n" \
  "s_add_u32 s60, s55, 0\n" \
  "s_add_u32 m0, s73, 0\n" \
  "s_nop 0\n" \
  "buffer_load_dwordx4 v54, s[40:43], s60 offen offset:0 nt lds\n" \
  "v_and_b32 v54, 0x38, v0\n" \
  "v_lshlrev_b32 v54, 7, v54\n" \
  "v_sub_u32 v54, v1, v54\n" \
  "s_add_u32 s60, s55, 0\n" \
  "s_add_u32 m0, s73, 896\n" \
  "s_nop 0\n" \
  "buffer_load_dwordx4 v54, s[40:43], s60 offen offset:128 nt lds\n" \
  "v_and_b32 v54, 0x38, v0\n" \
  "v_lshlrev_b32 v54, 7, v54\n" \
  "v_sub_u32 v54, v1, v54\n" \
  "s_add_u32 s60, s55, 0\n" \
  "s_add_u32 m0, s73, 1792\n" \
  "s_nop 0\n" \
  "buffer_load_dwordx4 v54, s[40:43], s60 offen offset:256 nt lds\n" \
  "v_and_b32 v54, 0x38, v0\n" \
  "v_lshlrev_b32 v54, 7, v54\n" \
  "v_sub_u32 v54, v1, v54\n" \
  "s_add_u32 s60, s55, 0\n" \
  "s_add_u32 m0, s73, 2688\n" \
  "s_nop 0\n" \
  "buffer_load_dwordx4 v54, s[40:43], s60 offen offset:384 nt lds\n" \
  "v_and_b32 v54, 0xc0, v0\n" \
  "v_lshlrev_b32 v54, 4, v54\n" \
  "v_sub_u32 v54, v2, v54\n" \
  "s_add_u32 s60, s55, 0\n" \
  "s_add_u32 m0, s73, 4096\n" \
  "s_nop 0\n" \
  "buffer_load_dwordx4 v54, s[44:47], s60 offen offset:0 nt lds\n" \
  "v_and_b32 v54, 0xc0, v0\n" \
  "v_lshlrev_b32 v54, 4, v54\n" \
  "v_sub_u32 v54, v2, v54\n" \
  "s_add_u32 s60, s55, 32768\n" \
  "s_add_u32 m0, s73, 5120\n" \
  "s_nop 0\n" \
  "buffer_load_dwordx4 v54, s[44:47], s60 offen offset:0 nt lds\n" \
  "v_and_b32 v54, 0xc0, v0\n" \
  "v_lshlrev_b32 v54, 4, v54\n" \
  "v_sub_u32 v54, v2, v54\n" \
  "s_add_u32 s60, s55, 0\n" \
  "s_add_u32 m0, s73, 5888\n" \
  "s_nop 0\n" \
  "buffer_load_dwordx4 v54, s[44:47], s60 offen offset:256 nt lds\n" \
  "v_and_b32 v54, 0xc0, v0\n" \
  "v_lshlrev_b32 v54, 4, v54\n" \
  "v_sub_u32 v54, v2, v54\n" \
  "s_add_u32 s60, s55, 32768\n" \
  "s_add_u32 m0, s73, 6912\n" \
  "s_nop 0\n" \
  "buffer_load_dwordx4 v54, s[44:47], s60 offen offset:256 nt lds\n" \
  "LM_17_%=:\n" \
  "s_add_u32 s60, s53, 6144\n" \
  "buffer_load_dwordx4 a[128:131], v1, s[40:43], s60 offen offset:0 nt\n" \
  "s_add_u32 s60, s53, 6144\n" \
  "buffer_load_dwordx4 a[132:135], v1, s[40:43], s60 offen offset:128 nt\n" \
  "s_add_u32 s60, s53, 6144\n" \
  "buffer_load_dwordx4 a[136:139], v1, s[40:43], s60 offen offset:256 nt\n" \
  "s_add_u32 s60, s53, 6144\n" \
  "buffer_load_dwordx4 a[140:143], v1, s[40:43], s60 offen offset:384 nt\n" \
  "s_add_u32 s60, s53, 6144\n" \
  "buffer_load_dwordx4 a[144:147], v2, s[44:47], s60 offen offset:0 nt\n" \
  "s_add_u32 s60, s53, 43008\n" \
  "buffer_load_dwordx4 a[148:151], v2, s[44:47], s60 offen offset:0 nt\n" \
  "s_add_u32 s60, s53, 6144\n" \
  "buffer_load_dwordx4 a[152:155], v2, s[44:47], s60 offen offset:256 nt\n" \
  "s_add_u32 s60, s53, 43008\n" \
  "buffer_load_dwordx4 a[156:159], v2, s[44:47], s60 offen offset:256 nt\n" \
  "s_add_u32 s60, s53, 6656\n" \
  "buffer_load_dwordx4 a[160:163], v1, s[40:43], s60 offen offset:0 nt\n" \
  "s_add_u32 s60, s53, 6656\n" \
  "buffer_load_dwordx4 a[164:167], v1, s[40:43], s60 offen offset:128 nt\n" \
  "s_add_u32 s60, s53, 6656\n" \
  "buffer_load_dwordx4 a[168:171], v1, s[40:43], s60 offen offset:256 nt\n" \
  "s_add_u32 s60, s53, 6656\n" \
  "buffer_load_dwordx4 a[172:175], v1, s[40:43], s60 offen offset:384 nt\n" \
  "s_add_u32 s60, s53, 6656\n" \
  "buffer_load_dwordx4 a[176:179], v2, s[44:47], s60 offen offset:0 nt\n" \
  "s_add_u32 s60, s53, 43520\n" \
  "buffer_load_dwordx4 a[180:183], v2, s[44:47], s60 offen offset:0 nt\n" \
  "s_add_u32 s60, s53, 6656\n" \
  "buffer_load_dwordx4 a[184:187], v2, s[44:47], s60 offen offset:256 nt\n" \
  "s_add_u32 s60, s53, 43520\n" \
  "buffer_load_dwordx4 a[188:191], v2, s[44:47], s60 offen offset:256 nt\n" \
  "s_add_u32 s60, s53, 7168\n" \
  "buffer_load_dwordx4 a[192:195], v1, s[40:43], s60 offen offset:0 nt\n" \
  "s_add_u32 s60, s53, 7168\n" \
  "buffer_load_dwordx4 a[196:199], v1, s[40:43], s60 offen offset:128 nt\n" \
  "s_add_u32 s60, s53, 7168\n" \
  "buffer_load_dwordx4 a[200:203], v1, s[40:43], s60 offen offset:256 nt\n" \
  "s_add_u32 s60, s53, 7168\n" \
  "buffer_load_dwordx4 a[204:207], v1, s[40:43], s60 offen offset:384 nt\n" \
  "s_add_u32 s60, s53, 7168\n" \
  "buffer_load_dwordx4 a[208:211], v2, s[44:47], s60 offen offset:0 nt\n" \
  "s_add_u32 s60, s53, 44032\n" \
  "buffer_load_dwordx4 a[212:215], v2, s[44:47], s60 offen offset:0 nt\n" \
  "s_add_u32 s60, s53, 7168\n" \
  "buffer_load_dwordx4 a[216:219], v2, s[44:47], s60 offen offset:256 nt\n" \
  "s_add_u32 s60, s53, 44032\n" \
  "buffer_load_dwordx4 a[220:223], v2, s[44:47], s60 offen offset:256 nt\n" \
  "s_waitcnt vmcnt(47)\n" \
  "s_add_u32 s60, s53, 7680\n" \
  "buffer_load_dwordx4 a[240:243], v2, s[44:47], s60 offen offset:0 nt\n" \
  "s_add_u32 s60, s53, 44544\n" \
  "buffer_load_dwordx4 a[244:247], v2, s[44:47], s60 offen offset:0 nt\n" \
  "s_add_u32 s60, s53, 7680\n" \
  "buffer_load_dwordx4 a[248:251], v2, s[44:47], s60 offen offset:256 nt\n" \
  "s_add_u32 s60, s53, 44544\n" \
  "buffer_load_dwordx4 a[252:255], v2, s[44:47], s60 offen offset:256 nt\n" \
  "s_waitcnt vmcnt(47)\n" \
  "s_add_u32 s60, s53, 8192\n" \
  "buffer_load_dwordx4 v[64:67], v1, s[40:43], s60 offen offset:0 nt\n" \
  "s_add_u32 s60, s53, 8192\n" \
  "buffer_load_dwordx4 v[68:71], v1, s[40:43], s60 offen offset:128 nt\n" \
  "s_add_u32 s60, s53, 8192\n" \
  "buffer_load_dwordx4 v[72:75], v1, s[40:43], s60 offen offset:256 nt\n" \
  "s_add_u32 s60, s53, 8192\n" \
  "buffer_load_dwordx4 v[76:79], v1, s[40:43], s60 offen offset:384 nt\n" \
  "s_add_u32 s60, s53, 8192\n" \
  "buffer_load_dwordx4 v[80:83], v2, s[44:47], s60 offen offset:0 nt\n" \
  "s_add_u32 s60, s53, 45056\n" \
  "buffer_load_dwordx4 v[84:87], v2, s[44:47], s60 offen offset:0 nt\n" \
  "s_add_u32 s60, s53, 8192\n" \
  "buffer_load_dwordx4 v[88:91], v2, s[44:47], s60 offen offset:256 nt\n" \
  "s_add_u32 s60, s53, 45056\n" \
  "buffer_load_dwordx4 v[92:95], v2, s[44:47], s60 offen offset:256 nt\n" \
  "s_waitcnt vmcnt(47)\n" \
  "s_add_u32 s60, s53, 8704\n" \
  "buffer_load_dwordx4 v[96:99], v1, s[40:43], s60 offen offset:0 nt\n" \
  "s_add_u32 s60, s53, 8704\n" \
  "buffer_load_dwordx4 v[100:103], v1, s[40:43], s60 offen offset:128 nt\n" \
  "s_add_u32 s60, s53, 8704\n" \
  "buffer_load_dwordx4 v[104:107], v1, s[40:43], s60 offen offset:256 nt\n" \
  "s_add_u32 s60, s53, 8704\n" \
  "buffer_load_dwordx4 v[108:111], v1, s[40:43], s60 offen offset:384 nt\n" \
  "s_add_u32 s60, s53, 8704\n" \
  "buffer_load_dwordx4 v[112:115], v2, s[44:47], s60 offen offset:0 nt\n" \
  "s_add_u32 s60, s53, 45568\n" \
  "buffer_load_dwordx4 v[116:119], v2, s[44:47], s60 offen offset:0 nt\n" \
  "s_add_u32 s60, s53, 8704\n" \
  "buffer_load_dwordx4 v[120:123], v2, s[44:47], s60 offen offset:256 nt\n" \
  "s_add_u32 s60, s53, 45568\n" \
  "buffer_load_dwordx4 v[124:127], v2, s[44:47], s60 offen offset:256 nt\n" \
  "s_waitcnt vmcnt(48)\n" \
  "s_waitcnt vmcnt(44)\n" \
  "s_cmp_eq_u32 s56, 0\n" \
  "s_cbranch_scc1 LM_18_%=\n" \
  "s_waitcnt vmcnt(44)\n" \
  "v_lshlrev_b32 v55, 2, v0\n" \
  "v_add_u32 v55, s73, v55\n" \
  "ds_read_b128 a[0:3], v55 offset:0\n" \
  "ds_read_b128 a[4:7], v55 offset:1024\n" \
  "ds_read_b128 a[8:11], v55 offset:2048\n" \
  "ds_read_b128 a[12:15], v55 offset:3072\n" \
  "ds_read_b128 a[16:19], v55 offset:4096\n" \
  "ds_read_b128 a[20:23], v55 offset:5120\n" \
  "ds_read_b128 a[24:27], v55 offset:6144\n" \
  "ds_read_b128 a[28:31], v55 offset:7168\n" \
  "s_waitcnt lgkmcnt(0)\n" \
  "LM_18_%=:\n" \
  "s_mul_i32 s62, s56, 0xaaaa\n" \
  "s_mov_b32 s63, 0xaaaaaaaa\n" \
  "s_call_b64 s[66:67], LC40_%=\n" \
  "s_nop 1\n" \
  "s_waitcnt vmcnt(36)\n" \
  "s_waitcnt vmcnt(28)\n" \
  "s_waitcnt vmcnt(20)\n" \
  "s_waitcnt vmcnt(16)\n" \
  "s_mov_b32 s62, 0xaaaaaaaa\n" \
  "s_mov_b32 s63, 0xaaaaaaaa\n" \
  "s_call_b64 s[66:67], LC41_%=\n" \
  "s_nop 1\n" \
  "s_waitcnt vmcnt(8)\n" \
  "s_waitcnt vmcnt(0)\n" \
  "s_mov_b32 s62, 0xaaaaaaaa\n" \
  "s_mov_b32 s63, 0x0\n" \
  "s_call_b64 s[66:67], LC42_%=\n" \
  "s_nop 1\n" \
  "s_add_u32 s70, s58, 243712\n" \
  "s_mov_b32 s71, 151552\n" \
  "s_mov_b32 s77, 155680\n" \
  "s_waitcnt lgkmcnt(0)\n" \
  "s_call_b64 s[66:67], LDU_%=\n" \
  "s_nop 1\n" \
  "s_memrealtime s[90:91]\n" \
  "s_add_u32 s60, s52, 8192\n" \
  "buffer_load_dwordx4 a[0:3], v1, s[40:43], s60 offen offset:0 nt\n" \
  "s_add_u32 s60, s52, 8192\n" \
  "buffer_load_dwordx4 a[4:7], v1, s[40:43], s60 offen offset:128 nt\n" \
  "s_add_u32 s60, s52, 8192\n" \
  "buffer_load_dwordx4 a[8:11], v1, s[40:43], s60 offen offset:256 nt\n" \
  "s_add_u32 s60, s52, 8192\n" \
  "buffer_load_dwordx4 a[12:15], v1, s[40:43], s60 offen offset:384 nt\n" \
  "s_add_u32 s60, s52, 8192\n" \
  "buffer_load_dwordx4 a[16:19], v2, s[44:47], s60 offen offset:0 nt\n" \
  "s_add_u32 s60, s52, 45056\n" \
  "buffer_load_dwordx4 a[20:23], v2, s[44:47], s60 offen offset:0 nt\n" \
  "s_add_u32 s60, s52, 8192\n" \
  "buffer_load_dwordx4 a[24:27], v2, s[44:47], s60 offen offset:256 nt\n" \
  "s_add_u32 s60, s52, 45056\n" \
  "buffer_load_dwordx4 a[28:31], v2, s[44:47], s60 offen offset:256 nt\n" \
  "s_add_u32 s60, s52, 8704\n" \
  "buffer_load_dwordx4 a[32:35], v1, s[40:43], s60 offen offset:0 nt\n" \
  "s_add_u32 s60, s52, 8704\n" \
  "buffer_load_dwordx4 a[36:39], v1, s[40:43], s60 offen offset:128 nt\n" \
  "s_add_u32 s60, s52, 8704\n" \
  "buffer_load_dwordx4 a[40:43], v1, s[40:43], s60 offen offset:256 nt\n" \
  "s_add_u32 s60, s52, 8704\n" \
  "buffer_load_dwordx4 a[44:47], v1, s[40:43], s60 offen offset:384 nt\n" \
  "s_add_u32 s60, s52, 8704\n" \
  "buffer_load_dwordx4 a[48:51], v2, s[44:47], s60 offen offset:0 nt\n" \
  "s_add_u32 s60, s52, 45568\n" \
  "buffer_load_dwordx4 a[52:55], v2, s[44:47], s60 offen offset:0 nt\n" \
  "s_add_u32 s60, s52, 8704\n" \
  "buffer_load_dwordx4 a[56:59], v2, s[44:47], s60 offen offset:256 nt\n" \
  "s_add_u32 s60, s52, 45568\n" \
  "buffer_load_dwordx4 a[60:63], v2, s[44:47], s60 offen offset:256 nt\n" \
  "s_add_u32 s60, s53, 0\n" \
  "buffer_load_dwordx4 v[128:131], v1, s[40:43], s60 offen offset:0 nt\n" \
  "s_add_u32 s60, s53, 0\n" \
  "buffer_load_dwordx4 v[132:135], v1, s[40:43], s60 offen offset:128 nt\n" \
  "s_add_u32 s60, s53, 0\n" \
  "buffer_load_dwordx4 v[136:139], v1, s[40:43], s60 offen offset:256 nt\n" \
  "s_add_u32 s60, s53, 0\n" \
  "buffer_load_dwordx4 v[140:143], v1, s[40:43], s60 offen offset:384 nt\n" \
  "s_add_u32 s60, s53, 0\n" \
  "buffer_load_dwordx4 v[144:147], v2, s[44:47], s60 offen offset:0 nt\n" \
  "s_add_u32 s60, s53, 36864\n" \
  "buffer_load_dwordx4 v[148:151], v2, s[44:47], s60 offen offset:0 nt\n" \
  "s_add_u32 s60, s53, 0\n" \
  "buffer_load_dwordx4 v[152:155], v2, s[44:47], s60 offen offset:256 nt\n" \
  "s_add_u32 s60, s53, 36864\n" \
  "buffer_load_dwordx4 v[156:159], v2, s[44:47], s60 offen offset:256 nt\n" \
  "s_add_u32 s60, s53, 512\n" \
  "buffer_load_dwordx4 v[160:163], v1, s[40:43], s60 offen offset:0 nt\n" \
  "s_add_u32 s60, s53, 512\n" \
  "buffer_load_dwordx4 v[164:167], v1, s[40:43], s60 offen offset:128 nt\n" \
  "s_add_u32 s60, s53, 512\n" \
  "buffer_load_dwordx4 v[168:171], v1, s[40:43], s60 offen offset:256 nt\n" \
  "s_add_u32 s60, s53, 512\n" \
  "buffer_load_dwordx4 v[172:175], v1, s[40:43], s60 offen offset:384 nt\n" \
  "s_add_u32 s60, s53, 512\n" \
  "buffer_load_dwordx4 v[176:179], v2, s[44:47], s60 offen offset:0 nt\n" \
  "s_add_u32 s60, s53, 37376\n" \
  "buffer_load_dwordx4 v[180:183], v2, s[44:47], s60 offen offset:0 nt\n" \
  "s_add_u32 s60, s53, 512\n" \
  "buffer_load_dwordx4 v[184:187], v2, s[44:47], s60 offen offset:256 nt\n" \
  "s_add_u32 s60, s53, 37376\n" \
  "buffer_load_dwordx4 v[188:191], v2, s[44:47], s60 offen offset:256 nt\n" \
  "s_add_u32 s60, s53, 1024\n" \
  "s_add_u32 m0, s69, 16384\n" \
  "s_nop 0\n" \
  "buffer_load_dwordx4 v1, s[40:43], s60 offen offset:0 nt lds\n" \
  "s_add_u32 s60, s53, 1024\n" \
  "s_add_u32 m0, s69, 17280\n" \
  "s_nop 0\n" \
  "buffer_load_dwordx4 v1, s[40:43], s60 offen offset:128 nt lds\n" \
  "s_add_u32 s60, s53, 1024\n" \
  "s_add_u32 m0, s69, 18176\n" \
  "s_nop 0\n" \
  "buffer_load_dwordx4 v1, s[40:43], s60 offen offset:256 nt lds\n" \
  "s_add_u32 s60, s53, 1024\n" \
  "s_add_u32 m0, s69, 19072\n" \
  "s_nop 0\n" \
  "buffer_load_dwordx4 v1, s[40:43], s60 offen offset:384 nt lds\n" \
  "s_add_u32 s60, s53, 1024\n" \
  "s_add_u32 m0, s69, 20480\n" \
  "s_nop 0\n" \
  "buffer_load_dwordx4 v2, s[44:47], s60 offen offset:0 nt lds\n" \
  "s_add_u32 s60, s53, 37888\n" \
  "s_add_u32 m0, s69, 21504\n" \
  "s_nop 0\n" \
  "buffer_load_dwordx4 v2, s[44:47], s60 offen offset:0 nt lds\n" \
  "s_add_u32 s60, s53, 1024\n" \
  "s_add_u32 m0, s69, 22272\n" \
  "s_nop 0\n" \
  "buffer_load_dwordx4 v2, s[44:47], s60 offen offset:256 nt lds\n" \
  "s_add_u32 s60, s53, 37888\n" \
  "s_add_u32 m0, s69, 23296\n" \
  "s_nop 0\n" \
  "buffer_load_dwordx4 v2, s[44:47], s60 offen offset:256 nt lds\n" \
  "ds_read_b128 v[24:27], v3 offset:24576\n" \
  "ds_read_b128 v[28:31], v3 offset:25600\n" \
  "ds_read_b64 v[6:7], v3 offset:26624\n" \
  "s_waitcnt lgkmcnt(0)\n" \
  "s_mov_b32 s70, 161792\n" \
  "s_add_u32 s71, s82, 32768\n" \
  "s_waitcnt lgkmcnt(0)\n" \
  "s_call_b64 s[66:67], LQL_%=\n" \
  "s_nop 1\n" \
  "s_waitcnt vmcnt(24)\n" \
  "s_mov_b32 s62, 0xaaaaaaaa\n" \
  "s_mov_b32 s63, 0x0\n" \
  "s_call_b64 s[66:67], LC40_%=\n" \
  "s_nop 1\n" \
  "s_add_u32 s70, s58, 487424\n" \
  "s_mov_b32 s71, 147456\n" \
  "s_mov_b32 s77, 155648\n" \
  "s_waitcnt lgkmcnt(0)\n" \
  "s_call_b64 s[66:67], LDU_%=\n" \
  "s_nop 1\n" \
  "s_add_u32 s60, s53, 3072\n" \
  "s_add_u32 m0, s69, 0\n" \
  "s_nop 0\n" \
  "buffer_load_dwordx4 v1, s[40:43], s60 offen offset:0 nt lds\n" \
  "s_add_u32 s60, s53, 3072\n" \
  "s_add_u32 m0, s69, 896\n" \
  "s_nop 0\n" \
  "buffer_load_dwordx4 v1, s[40:43], s60 offen offset:128 nt lds\n" \
  "s_add_u32 s60, s53, 3072\n" \
  "s_add_u32 m0, s69, 1792\n" \
  "s_nop 0\n" \
  "buffer_load_dwordx4 v1, s[40:43], s60 offen offset:256 nt lds\n" \
  "s_add_u32 s60, s53, 3072\n" \
  "s_add_u32 m0, s69, 2688\n" \
  "s_nop 0\n" \
  "buffer_load_dwordx4 v1, s[40:43], s60 offen offset:384 nt lds\n" \
  "s_add_u32 s60, s53, 3072\n" \
  "s_add_u32 m0, s69, 4096\n" \
  "s_nop 0\n" \
  "buffer_load_dwordx4 v2, s[44:47], s60 offen offset:0 nt lds\n" \
  "s_add_u32 s60, s53, 39936\n" \
  "s_add_u32 m0, s69, 5120\n" \
  "s_nop 0\n" \
  "buffer_load_dwordx4 v2, s[44:47], s60 offen offset:0 nt lds\n" \
  "s_add_u32 s60, s53, 3072\n" \
  "s_add_u32 m0, s69, 5888\n" \
  "s_nop 0\n" \
  "buffer_load_dwordx4 v2, s[44:47], s60 offen offset:256 nt lds\n" \
  "s_add_u32 s60, s53, 39936\n" \
  "s_add_u32 m0, s69, 6912\n" \
  "s_nop 0\n" \
  "buffer_load_dwordx4 v2, s[44:47], s60 offen offset:256 nt lds\n" \
  "s_add_u32 s60, s53, 3584\n" \
  "s_add_u32 m0, s69, 8192\n" \
  "s_nop 0\n" \
  "buffer_load_dwordx4 v1, s[40:43], s60 offen offset:0 nt lds\n" \
  "s_add_u32 s60, s53, 3584\n" \
  "s_add_u32 m0, s69, 9088\n" \
  "s_nop 0\n" \
  "buffer_load_dwordx4 v1, s[40:43], s60 offen offset:128 nt lds\n" \
  "s_add_u32 s60, s53, 3584\n" \
  "s_add_u32 m0, s69, 9984\n" \
  "s_nop 0\n" \
  "buffer_load_dwordx4 v1, s[40:43], s60 offen offset:256 nt lds\n" \
  "s_add_u32 s60, s53, 3584\n" \
  "s_add_u32 m0, s69, 10880\n" \
  "s_nop 0\n" \
  "buffer_load_dwordx4 v1, s[40:43], s60 offen offset:384 nt lds\n" \
  "s_add_u32 s60, s53, 3584\n" \
  "s_add_u32 m0, s69, 12288\n" \
  "s_nop 0\n" \
  "buffer_load_dwordx4 v2, s[44:47], s60 offen offset:0 nt lds\n" \
  "s_add_u32 s60, s53, 40448\n" \
  "s_add_u32 m0, s69, 13312\n" \
  "s_nop 0\n" \
  "buffer_load_dwordx4 v2, s[44:47], s60 offen offset:0 nt lds\n" \
  "s_add_u32 s60, s53, 3584\n" \
  "s_add_u32 m0, s69, 14080\n" \
  "s_nop 0\n" \
  "buffer_load_dwordx4 v2, s[44:47], s60 offen offset:256 nt lds\n" \
  "s_add_u32 s60, s53, 40448\n" \
  "s_add_u32 m0, s69, 15104\n" \
  "s_nop 0\n" \
  "buffer_load_dwordx4 v2, s[44:47], s60 offen offset:256 nt lds\n" \
  "LM_19_%=:\n" \
  "s_memrealtime s[84:85]\n" \
  "s_waitcnt lgkmcnt(0)\n" \
  "s_sub_u32 s84, s84, s90\n" \
  "s_cmp_ge_u32 s84, 300\n" \
  "s_cbranch_scc1 LM_20_%=\n" \
  "s_sleep 1\n" \
  "s_branch LM_19_%=\n" \
  "LM_20_%=:\n" \
  "s_add_u32 s60, s53, 1536\n" \
  "s_add_u32 m0, s69, 24576\n" \
  "s_nop 0\n" \
  "buffer_load_dwordx4 v1, s[40:43], s60 offen offset:0 nt lds\n" \
  "s_add_u32 s60, s53, 1536\n" \
  "s_add_u32 m0, s69, 25472\n" \
  "s_nop 0\n" \
  "buffer_load_dwordx4 v1, s[40:43], s60 offen offset:128 nt lds\n" \
  "s_add_u32 s60, s53, 1536\n" \
  "s_add_u32 m0, s69, 26368\n" \
  "s_nop 0\n" \
  "buffer_load_dwordx4 v1, s[40:43], s60 offen offset:256 nt lds\n" \
  "s_add_u32 s60, s53, 1536\n" \
  "s_add_u32 m0, s69, 27264\n" \
  "s_nop 0\n" \
  "buffer_load_dwordx4 v1, s[40:43], s60 offen offset:384 nt lds\n" \
  "s_add_u32 s60, s53, 1536\n" \
  "s_add_u32 m0, s69, 28672\n" \
  "s_nop 0\n" \
  "buffer_load_dwordx4 v2, s[44:47], s60 offen offset:0 nt lds\n" \
  "s_add_u32 s60, s53, 38400\n" \
  "s_add_u32 m0, s69, 29696\n" \
  "s_nop 0\n" \
  "buffer_load_dwordx4 v2, s[44:47], s60 offen offset:0 nt lds\n" \
  "s_add_u32 s60, s53, 1536\n" \
  "s_add_u32 m0, s69, 30464\n" \
  "s_nop 0\n" \
  "buffer_load_dwordx4 v2, s[44:47], s60 offen offset:256 nt lds\n" \
  "s_add_u32 s60, s53, 38400\n" \
  "s_add_u32 m0, s69, 31488\n" \
  "s_nop 0\n" \
  "buffer_load_dwordx4 v2, s[44:47], s60 offen offset:256 nt lds\n" \
  "s_add_u32 m0, s92, 162816\n" \
  "s_add_u32 s60, s57, 49152\n" \
  "s_mov_b32 exec_hi, 0\n" \
  "buffer_load_dword v0, s[48:51], s60 offen sc1 lds\n" \
  "s_mov_b32 exec_hi, -1\n" \
  "LM_21_%=:\n" \
  "s_memrealtime s[84:85]\n" \
  "s_waitcnt lgkmcnt(0)\n" \
  "s_sub_u32 s84, s84, s90\n" \
  "s_cmp_ge_u32 s84, 400\n" \
  "s_cbranch_scc1 LM_22_%=\n" \
  "s_sleep 1\n" \
  "s_branch LM_21_%=\n" \
  "LM_22_%=:\n" \
  "s_add_u32 s60, s53, 2048\n" \
  "buffer_load_dwordx4 a[32:35], v1, s[40:43], s60 offen offset:0 nt\n" \
  "s_add_u32 s60, s53, 2048\n" \
  "buffer_load_dwordx4 a[36:39], v1, s[40:43], s60 offen offset:128 nt\n" \
  "s_add_u32 s60, s53, 2048\n" \
  "buffer_load_dwordx4 a[40:43], v1, s[40:43], s60 offen offset:256 nt\n" \
  "s_add_u32 s60, s53, 2048\n" \
  "buffer_load_dwordx4 a[44:47], v1, s[40:43], s60 offen offset:384 nt\n" \
  "s_add_u32 s60, s53, 2048\n" \
  "buffer_load_dwordx4 a[48:51], v2, s[44:47], s60 offen offset:0 nt\n" \
  "s_add_u32 s60, s53, 38912\n" \
  "buffer_load_dwordx4 a[52:55], v2, s[44:47], s60 offen offset:0 nt\n" \
  "s_add_u32 s60, s53, 2048\n" \
  "buffer_load_dwordx4 a[56:59], v2, s[44:47], s60 offen offset:256 nt\n" \
  "s_add_u32 s60, s53, 38912\n" \
  "buffer_load_dwordx4 a[60:63], v2, s[44:47], s60 offen offset:256 nt\n" \
  "s_add_u32 m0, s92, 162816\n" \
  "s_add_u32 s60, s57, 49152\n" \
  "s_mov_b32 exec_hi, 0\n" \
  "buffer_load_dword v0, s[48:51], s60 offen sc1 lds\n" \
  "s_mov_b32 exec_hi, -1\n" \
  "LM_23_%=:\n" \
  "s_memrealtime s[84:85]\n" \
  "s_waitcnt lgkmcnt(0)\n" \
  "s_sub_u32 s84, s84, s90\n" \
  "s_cmp_ge_u32 s84, 500\n" \
  "s_cbranch_scc1 LM_24_%=\n" \
  "s_sleep 1\n" \
  "s_branch LM_23_%=\n" \
  "LM_24_%=:\n" \
  "s_waitcnt vmcnt(55)\n" \
  "s_add_u32 s60, s53, 2560\n" \
  "buffer_load_dwordx4 a[0:3], v1, s[40:43], s60 offen offset:0 nt\n" \
  "s_add_u32 s60, s53, 2560\n" \
  "buffer_load_dwordx4 a[4:7], v1, s[40:43], s60 offen offset:128 nt\n" \
  "s_add_u32 s60, s53, 2560\n" \
  "buffer_load_dwordx4 a[8:11], v1, s[40:43], s60 offen offset:256 nt\n" \
  "s_add_u32 s60, s53, 2560\n" \
  "buffer_load_dwordx4 a[12:15], v1, s[40:43], s60 offen offset:384 nt\n" \
  "s_add_u32 s60, s53, 2560\n" \
  "buffer_load_dwordx4 a[16:19], v2, s[44:47], s60 offen offset:0 nt\n" \
  "s_add_u32 s60, s53, 39424\n" \
  "buffer_load_dwordx4 a[20:23], v2, s[44:47], s60 offen offset:0 nt\n" \
  "s_add_u32 s60, s53, 2560\n" \
  "buffer_load_dwordx4 a[24:27], v2, s[44:47], s60 offen offset:256 nt\n" \
  "s_add_u32 s60, s53, 39424\n" \
  "buffer_load_dwordx4 a[28:31], v2, s[44:47], s60 offen offset:256 nt\n" \
  "s_add_u32 m0, s92, 162816\n" \
  "s_add_u32 s60, s57, 49152\n" \
  "s_mov_b32 exec_hi, 0\n" \
  "buffer_load_dword v0, s[48:51], s60 offen sc1 lds\n" \
  "s_mov_b32 exec_hi, -1\n" \
  "s_branch Lph6_%=\n" \
  "Lph6_%=:\n" \
  "s_nop 1\n" \
  "s_mov_b32 s70, 162816\n" \
  "s_add_u32 s71, s82, 49152\n" \
  "s_waitcnt lgkmcnt(0)\n" \
  "s_call_b64 s[66:67], LQL_%=\n" \
  "s_nop 1\n" \
  "s_waitcnt vmcnt(0)\n" \
  "s_mov_b32 s76, 1\n" \
  "s_mul_i32 s78, s56, 0xaaaa\n" \
  "s_or_b32 s78, s78, 0xaaaa0000\n" \
  "s_mov_b32 s79, 0xaaaa\n" \
  "s_waitcnt lgkmcnt(0)\n" \
  "s_call_b64 s[66:67], LB1_%=\n" \
  "s_nop 1\n" \
  "s_call_b64 s[66:67], LB3a_%=\n" \
  "s_nop 1\n" \
  "s_call_b64 s[66:67], LB3b_%=\n" \
  "s_nop 1\n" \
  "s_add_u32 s70, s58, 731136\n" \
  "s_mov_b32 s71, 151552\n" \
  "s_mov_b32 s77, 155680\n" \
  "s_waitcnt lgkmcnt(0)\n" \
  "s_call_b64 s[66:67], LDU_%=\n" \
  "s_nop 1\n" \
  "s_branch Lend_%=\n" \
  "LCW0_%=:\n" \
  "s_nop 1\n" \
  "v_mov_b64 v[32:33], 0\n" \
  "v_mov_b64 v[34:35], 0\n" \
  "v_mov_b64 v[36:37], 0\n" \
  "v_mov_b64 v[38:39], 0\n" \
  "v_smfmac_f32_16x16x64_bf16 v[32:35], v[8:11], v[192:199], v4\n" \
  "v_smfmac_f32_16x16x64_bf16 v[32:35], v[12:15], v[200:207], v4\n" \
  "s_nop 7\n" \
  "v_add_f32 v32, v32, v34\n" \
  "v_add_f32 v33, v33, v35\n" \
  "s_nop 0\n" \
  "v_add_f32_dpp v48, v32, v33 quad_perm:[1,0,3,2] row_mask:0xf bank_mask:0xf\n" \
  "v_cndmask_b32_e64 v48, v5, v48, s[62:63]\n" \
  "s_nop 1\n" \
  "v_max_f32_dpp v54, v48, v48 row_ror:2 row_mask:0xf bank_mask:0xf\n" \
  "s_nop 1\n" \
  "v_max_f32_dpp v54, v54, v54 row_ror:4 row_mask:0xf bank_mask:0xf\n" \
  "s_nop 1\n" \
  "v_max_f32_dpp v54, v54, v54 row_ror:8 row_mask:0xf bank_mask:0xf\n" \
  "s_nop 1\n" \
  "v_max_f32_dpp v54, v54, v54 row_bcast:15 row_mask:0xa bank_mask:0xf\n" \
  "s_nop 1\n" \
  "v_max_f32_dpp v54, v54, v54 row_bcast:31 row_mask:0xc bank_mask:0xf\n" \
  "s_nop 0\n" \
  "v_readlane_b32 s64, v54, 63\n" \
  "s_nop 1\n" \
  "v_max_f32 v54, s64, v6\n" \
  "v_sub_f32 v48, v48, v54\n" \
  "v_sub_f32 v46, v6, v54\n" \
  "v_exp_f32 v48, v48\n" \
  "v_exp_f32 v46, v46\n" \
  "v_mov_b32 v6, v54\n" \
  "v_cvt_pk_bf16_f32 v48, v48, 0\n" \
  "v_mul_f32 v7, v7, v46\n" \
  "v_dot2_f32_bf16 v7, v48, s65, v7\n" \
  "ds_bpermute_b32 v48, v53, v48\n" \
  "v_pk_mul_f32 v[24:25], v[24:25], v[46:47] op_sel_hi:[1,0]\n" \
  "v_pk_mul_f32 v[26:27], v[26:27], v[46:47] op_sel_hi:[1,0]\n" \
  "v_pk_mul_f32 v[28:29], v[28:29], v[46:47] op_sel_hi:[1,0]\n" \
  "v_pk_mul_f32 v[30:31], v[30:31], v[46:47] op_sel_hi:[1,0]\n" \
  "s_waitcnt lgkmcnt(0)\n" \
  "v_mov_b32_dpp v56, v48 row_newbcast:1 row_mask:0xf bank_mask:0x1\n" \
  "v_mov_b32_dpp v57, v48 row_newbcast:1 row_mask:0xf bank_mask:0x2\n" \
  "v_mov_b32_dpp v58, v48 row_newbcast:3 row_mask:0xf bank_mask:0x1\n" \
  "v_mov_b32_dpp v59, v48 row_newbcast:3 row_mask:0xf bank_mask:0x2\n" \
  "s_nop 1\n" \
  "v_smfmac_f32_16x16x64_bf16 v[24:27], v[56:59], v[208:215], v4 abid:1\n" \
  "v_smfmac_f32_16x16x64_bf16 v[28:31], v[56:59], v[216:223], v4 abid:1\n" \
  "s_nop 7\n" \
  "s_setpc_b64 s[66:67]\n" \
  "LCW1_%=:\n" \
  "s_nop 1\n" \
  "v_mov_b64 v[32:33], 0\n" \
  "v_mov_b64 v[34:35], 0\n" \
  "v_mov_b64 v[36:37], 0\n" \
  "v_mov_b64 v[38:39], 0\n" \
  "v_smfmac_f32_16x16x64_bf16 v[32:35], v[8:11], v[224:231], v4\n" \
  "v_smfmac_f32_16x16x64_bf16 v[32:35], v[12:15], v[232:239], v4\n" \
  "s_nop 7\n" \
  "v_add_f32 v32, v32, v34\n" \
  "v_add_f32 v33, v33, v35\n" \
  "s_nop 0\n" \
  "v_add_f32_dpp v48, v32, v33 quad_perm:[1,0,3,2] row_mask:0xf bank_mask:0xf\n" \
  "v_cndmask_b32_e64 v48, v5, v48, s[62:63]\n" \
  "s_nop 1\n" \
  "v_max_f32_dpp v54, v48, v48 row_ror:2 row_mask:0xf bank_mask:0xf\n" \
  "s_nop 1\n" \
  "v_max_f32_dpp v54, v54, v54 row_ror:4 row_mask:0xf bank_mask:0xf\n" \
  "s_nop 1\n" \
  "v_max_f32_dpp v54, v54, v54 row_ror:8 row_mask:0xf bank_mask:0xf\n" \
  "s_nop 1\n" \
  "v_max_f32_dpp v54, v54, v54 row_bcast:15 row_mask:0xa bank_mask:0xf\n" \
  "s_nop 1\n" \
  "v_max_f32_dpp v54, v54, v54 row_bcast:31 row_mask:0xc bank_mask:0xf\n" \
  "s_nop 0\n" \
  "v_readlane_b32 s64, v54, 63\n" \
  "s_nop 1\n" \
  "v_max_f32 v54, s64, v6\n" \
  "v_sub_f32 v48, v48, v54\n" \
  "v_sub_f32 v46, v6, v54\n" \
  "v_exp_f32 v48, v48\n" \
  "v_exp_f32 v46, v46\n" \
  "v_mov_b32 v6, v54\n" \
  "v_cvt_pk_bf16_f32 v48, v48, 0\n" \
  "v_mul_f32 v7, v7, v46\n" \
  "v_dot2_f32_bf16 v7, v48, s65, v7\n" \
  "ds_bpermute_b32 v48, v53, v48\n" \
  "v_pk_mul_f32 v[24:25], v[24:25], v[46:47] op_sel_hi:[1,0]\n" \
  "v_pk_mul_f32 v[26:27], v[26:27], v[46:47] op_sel_hi:[1,0]\n" \
  "v_pk_mul_f32 v[28:29], v[28:29], v[46:47] op_sel_hi:[1,0]\n" \
  "v_pk_mul_f32 v[30:31], v[30:31], v[46:47] op_sel_hi:[1,0]\n" \
  "s_waitcnt lgkmcnt(0)\n" \
  "v_mov_b32_dpp v56, v48 row_newbcast:1 row_mask:0xf bank_mask:0x1\n" \
  "v_mov_b32_dpp v57, v48 row_newbcast:1 row_mask:0xf bank_mask:0x2\n" \
  "v_mov_b32_dpp v58, v48 row_newbcast:3 row_mask:0xf bank_mask:0x1\n" \
  "v_mov_b32_dpp v59, v48 row_newbcast:3 row_mask:0xf bank_mask:0x2\n" \
  "s_nop 1\n" \
  "v_smfmac_f32_16x16x64_bf16 v[24:27], v[56:59], v[240:247], v4 abid:1\n" \
  "v_smfmac_f32_16x16x64_bf16 v[28:31], v[56:59], v[248:255], v4 abid:1\n" \
  "s_nop 7\n" \
  "s_setpc_b64 s[66:67]\n" \
  "LC40_%=:\n" \
  "s_nop 1\n" \
  "v_mov_b64 v[32:33], 0\n" \
  "v_mov_b64 v[34:35], 0\n" \
  "v_mov_b64 v[36:37], 0\n" \
  "v_mov_b64 v[38:39], 0\n" \
  "v_smfmac_f32_16x16x64_bf16 v[32:35], v[8:11], a[0:7], v4\n" \
  "v_smfmac_f32_16x16x64_bf16 v[32:35], v[12:15], a[8:15], v4\n" \
  "v_smfmac_f32_16x16x64_bf16 v[32:35], v[16:19], a[64:71], v4\n" \
  "v_smfmac_f32_16x16x64_bf16 v[32:35], v[20:23], a[72:79], v4\n" \
  "v_smfmac_f32_16x16x64_bf16 v[36:39], v[8:11], a[32:39], v4\n" \
  "v_smfmac_f32_16x16x64_bf16 v[36:39], v[12:15], a[40:47], v4\n" \
  "v_smfmac_f32_16x16x64_bf16 v[36:39], v[16:19], a[96:103], v4\n" \
  "v_smfmac_f32_16x16x64_bf16 v[36:39], v[20:23], a[104:111], v4\n" \
  "s_nop 3\n" \
  "v_add_f32 v32, v32, v34\n" \
  "v_add_f32 v33, v33, v35\n" \
  "s_nop 0\n" \
  "v_add_f32_dpp v48, v32, v33 quad_perm:[1,0,3,2] row_mask:0x5 bank_mask:0xf\n" \
  "v_add_f32 v36, v36, v38\n" \
  "v_add_f32 v37, v37, v39\n" \
  "s_nop 0\n" \
  "v_add_f32_dpp v48, v36, v37 quad_perm:[1,0,3,2] row_mask:0xa bank_mask:0xf\n" \
  "v_cndmask_b32_e64 v48, v5, v48, s[62:63]\n" \
  "s_nop 1\n" \
  "v_max_f32_dpp v54, v48, v48 row_ror:2 row_mask:0xf bank_mask:0xf\n" \
  "s_nop 1\n" \
  "v_max_f32_dpp v54, v54, v54 row_ror:4 row_mask:0xf bank_mask:0xf\n" \
  "s_nop 1\n" \
  "v_max_f32_dpp v54, v54, v54 row_ror:8 row_mask:0xf bank_mask:0xf\n" \
  "s_nop 1\n" \
  "v_max_f32_dpp v54, v54, v54 row_bcast:15 row_mask:0xa bank_mask:0xf\n" \
  "s_nop 1\n" \
  "v_max_f32_dpp v54, v54, v54 row_bcast:31 row_mask:0xc bank_mask:0xf\n" \
  "s_nop 0\n" \
  "v_readlane_b32 s64, v54, 63\n" \
  "s_nop 1\n" \
  "v_max_f32 v54, s64, v6\n" \
  "v_sub_f32 v48, v48, v54\n" \
  "v_sub_f32 v46, v6, v54\n" \
  "v_exp_f32 v48, v48\n" \
  "v_exp_f32 v46, v46\n" \
  "v_mov_b32 v6, v54\n" \
  "v_cvt_pk_bf16_f32 v48, v48, 0\n" \
  "v_mul_f32 v7, v7, v46\n" \
  "v_dot2_f32_bf16 v7, v48, s65, v7\n" \
  "ds_bpermute_b32 v48, v53, v48\n" \
  "v_pk_mul_f32 v[24:25], v[24:25], v[46:47] op_sel_hi:[1,0]\n" \
  "v_pk_mul_f32 v[26:27], v[26:27], v[46:47] op_sel_hi:[1,0]\n" \
  "v_pk_mul_f32 v[28:29], v[28:29], v[46:47] op_sel_hi:[1,0]\n" \
  "v_pk_mul_f32 v[30:31], v[30:31], v[46:47] op_sel_hi:[1,0]\n" \
  "s_waitcnt lgkmcnt(0)\n" \
  "v_mov_b64 v[32:33], 0\n" \
  "v_mov_b64 v[34:35], 0\n" \
  "v_mov_b32_dpp v56, v48 row_newbcast:1 row_mask:0xf bank_mask:0x1\n" \
  "v_mov_b32_dpp v57, v48 row_newbcast:1 row_mask:0xf bank_mask:0x2\n" \
  "v_mov_b32_dpp v58, v48 row_newbcast:3 row_mask:0xf bank_mask:0x1\n" \
  "v_mov_b32_dpp v59, v48 row_newbcast:3 row_mask:0xf bank_mask:0x2\n" \
  "v_mov_b32_dpp v60, v48 row_newbcast:5 row_mask:0xf bank_mask:0x1\n" \
  "v_mov_b32_dpp v61, v48 row_newbcast:5 row_mask:0xf bank_mask:0x2\n" \
  "v_mov_b32_dpp v62, v48 row_newbcast:7 row_mask:0xf bank_mask:0x1\n" \
  "v_mov_b32_dpp v63, v48 row_newbcast:7 row_mask:0xf bank_mask:0x2\n" \
  "v_smfmac_f32_16x16x64_bf16 v[24:27], v[56:59], a[16:23], v4 abid:1\n" \
  "v_mov_b32_dpp v32, v48 row_newbcast:9 row_mask:0xf bank_mask:0x1\n" \
  "v_mov_b32_dpp v33, v48 row_newbcast:9 row_mask:0xf bank_mask:0x2\n" \
  "v_smfmac_f32_16x16x64_bf16 v[28:31], v[56:59], a[24:31], v4 abid:1\n" \
  "v_mov_b32_dpp v34, v48 row_newbcast:11 row_mask:0xf bank_mask:0x1\n" \
  "v_mov_b32_dpp v35, v48 row_newbcast:11 row_mask:0xf bank_mask:0x2\n" \
  "v_smfmac_f32_16x16x64_bf16 v[24:27], v[60:63], a[48:55], v4 abid:1\n" \
  "v_mov_b32_dpp v56, v48 row_newbcast:13 row_mask:0xf bank_mask:0x1\n" \
  "v_mov_b32_dpp v57, v48 row_newbcast:13 row_mask:0xf bank_mask:0x2\n" \
  "v_smfmac_f32_16x16x64_bf16 v[28:31], v[60:63], a[56:63], v4 abid:1\n" \
  "v_mov_b32_dpp v58, v48 row_newbcast:15 row_mask:0xf bank_mask:0x1\n" \
  "v_mov_b32_dpp v59, v48 row_newbcast:15 row_mask:0xf bank_mask:0x2\n" \
  "v_smfmac_f32_16x16x64_bf16 v[24:27], v[32:35], a[80:87], v4 abid:1\n" \
  "v_smfmac_f32_16x16x64_bf16 v[28:31], v[32:35], a[88:95], v4 abid:1\n" \
  "v_smfmac_f32_16x16x64_bf16 v[24:27], v[56:59], a[112:119], v4 abid:1\n" \
  "v_smfmac_f32_16x16x64_bf16 v[28:31], v[56:59], a[120:127], v4 abid:1\n" \
  "s_nop 7\n" \
  "s_setpc_b64 s[66:67]\n" \
  "LC41_%=:\n" \
  "s_nop 1\n" \
  "v_mov_b64 v[32:33], 0\n" \
  "v_mov_b64 v[34:35], 0\n" \
  "v_mov_b64 v[36:37], 0\n" \
  "v_mov_b64 v[38:39], 0\n" \
  "v_smfmac_f32_16x16x64_bf16 v[32:35], v[8:11], a[128:135], v4\n" \
  "v_smfmac_f32_16x16x64_bf16 v[32:35], v[12:15], a[136:143], v4\n" \
  "v_smfmac_f32_16x16x64_bf16 v[32:35], v[16:19], a[192:199], v4\n" \
  "v_smfmac_f32_16x16x64_bf16 v[32:35], v[20:23], a[200:207], v4\n" \
  "v_smfmac_f32_16x16x64_bf16 v[36:39], v[8:11], a[160:167], v4\n" \
  "v_smfmac_f32_16x16x64_bf16 v[36:39], v[12:15], a[168:175], v4\n" \
  "v_smfmac_f32_16x16x64_bf16 v[36:39], v[16:19], a[224:231], v4\n" \
  "v_smfmac_f32_16x16x64_bf16 v[36:39], v[20:23], a[232:239], v4\n" \
  "s_nop 3\n" \
  "v_add_f32 v32, v32, v34\n" \
  "v_add_f32 v33, v33, v35\n" \
  "s_nop 0\n" \
  "v_add_f32_dpp v48, v32, v33 quad_perm:[1,0,3,2] row_mask:0x5 bank_mask:0xf\n" \
  "v_add_f32 v36, v36, v38\n" \
  "v_add_f32 v37, v37, v39\n" \
  "s_nop 0\n" \
  "v_add_f32_dpp v48, v36, v37 quad_perm:[1,0,3,2] row_mask:0xa bank_mask:0xf\n" \
  "v_cndmask_b32_e64 v48, v5, v48, s[62:63]\n" \
  "s_nop 1\n" \
  "v_max_f32_dpp v54, v48, v48 row_ror:2 row_mask:0xf bank_mask:0xf\n" \
  "s_nop 1\n" \
  "v_max_f32_dpp v54, v54, v54 row_ror:4 row_mask:0xf bank_mask:0xf\n" \
  "s_nop 1\n" \
  "v_max_f32_dpp v54, v54, v54 row_ror:8 row_mask:0xf bank_mask:0xf\n" \
  "s_nop 1\n" \
  "v_max_f32_dpp v54, v54, v54 row_bcast:15 row_mask:0xa bank_mask:0xf\n" \
  "s_nop 1\n" \
  "v_max_f32_dpp v54, v54, v54 row_bcast:31 row_mask:0xc bank_mask:0xf\n" \
  "s_nop 0\n" \
  "v_readlane_b32 s64, v54, 63\n" \
  "s_nop 1\n" \
  "v_max_f32 v54, s64, v6\n" \
  "v_sub_f32 v48, v48, v54\n" \
  "v_sub_f32 v46, v6, v54\n" \
  "v_exp_f32 v48, v48\n" \
  "v_exp_f32 v46, v46\n" \
  "v_mov_b32 v6, v54\n" \
  "v_cvt_pk_bf16_f32 v48, v48, 0\n" \
  "v_mul_f32 v7, v7, v46\n" \
  "v_dot2_f32_bf16 v7, v48, s65, v7\n" \
  "ds_bpermute_b32 v48, v53, v48\n" \
  "v_pk_mul_f32 v[24:25], v[24:25], v[46:47] op_sel_hi:[1,0]\n" \
  "v_pk_mul_f32 v[26:27], v[26:27], v[46:47] op_sel_hi:[1,0]\n" \
  "v_pk_mul_f32 v[28:29], v[28:29], v[46:47] op_sel_hi:[1,0]\n" \
  "v_pk_mul_f32 v[30:31], v[30:31], v[46:47] op_sel_hi:[1,0]\n" \
  "s_waitcnt lgkmcnt(0)\n" \
  "v_mov_b64 v[32:33], 0\n" \
  "v_mov_b64 v[34:35], 0\n" \
  "v_mov_b32_dpp v56, v48 row_newbcast:1 row_mask:0xf bank_mask:0x1\n" \
  "v_mov_b32_dpp v57, v48 row_newbcast:1 row_mask:0xf bank_mask:0x2\n" \
  "v_mov_b32_dpp v58, v48 row_newbcast:3 row_mask:0xf bank_mask:0x1\n" \
  "v_mov_b32_dpp v59, v48 row_newbcast:3 row_mask:0xf bank_mask:0x2\n" \
  "v_mov_b32_dpp v60, v48 row_newbcast:5 row_mask:0xf bank_mask:0x1\n" \
  "v_mov_b32_dpp v61, v48 row_newbcast:5 row_mask:0xf bank_mask:0x2\n" \
  "v_mov_b32_dpp v62, v48 row_newbcast:7 row_mask:0xf bank_mask:0x1\n" \
  "v_mov_b32_dpp v63, v48 row_newbcast:7 row_mask:0xf bank_mask:0x2\n" \
  "v_smfmac_f32_16x16x64_bf16 v[24:27], v[56:59], a[144:151], v4 abid:1\n" \
  "v_mov_b32_dpp v32, v48 row_newbcast:9 row_mask:0xf bank_mask:0x1\n" \
  "v_mov_b32_dpp v33, v48 row_newbcast:9 row_mask:0xf bank_mask:0x2\n" \
  "v_smfmac_f32_16x16x64_bf16 v[28:31], v[56:59], a[152:159], v4 abid:1\n" \
  "v_mov_b32_dpp v34, v48 row_newbcast:11 row_mask:0xf bank_mask:0x1\n" \
  "v_mov_b32_dpp v35, v48 row_newbcast:11 row_mask:0xf bank_mask:0x2\n" \
  "v_smfmac_f32_16x16x64_bf16 v[24:27], v[60:63], a[176:183], v4 abid:1\n" \
  "v_mov_b32_dpp v56, v48 row_newbcast:13 row_mask:0xf bank_mask:0x1\n" \
  "v_mov_b32_dpp v57, v48 row_newbcast:13 row_mask:0xf bank_mask:0x2\n" \
  "v_smfmac_f32_16x16x64_bf16 v[28:31], v[60:63], a[184:191], v4 abid:1\n" \
  "v_mov_b32_dpp v58, v48 row_newbcast:15 row_mask:0xf bank_mask:0x1\n" \
  "v_mov_b32_dpp v59, v48 row_newbcast:15 row_mask:0xf bank_mask:0x2\n" \
  "v_smfmac_f32_16x16x64_bf16 v[24:27], v[32:35], a[208:215], v4 abid:1\n" \
  "v_smfmac_f32_16x16x64_bf16 v[28:31], v[32:35], a[216:223], v4 abid:1\n" \
  "v_smfmac_f32_16x16x64_bf16 v[24:27], v[56:59], a[240:247], v4 abid:1\n" \
  "v_smfmac_f32_16x16x64_bf16 v[28:31], v[56:59], a[248:255], v4 abid:1\n" \
  "s_nop 7\n" \
  "s_setpc_b64 s[66:67]\n" \
  "LC42_%=:\n" \
  "s_nop 1\n" \
  "v_mov_b64 v[32:33], 0\n" \
  "v_mov_b64 v[34:35], 0\n" \
  "v_mov_b64 v[36:37], 0\n" \
  "v_mov_b64 v[38:39], 0\n" \
  "v_smfmac_f32_16x16x64_bf16 v[32:35], v[8:11], v[64:71], v4\n" \
  "v_smfmac_f32_16x16x64_bf16 v[32:35], v[12:15], v[72:79], v4\n" \
  "v_smfmac_f32_16x16x64_bf16 v[32:35], v[16:19], v[128:135], v4\n" \
  "v_smfmac_f32_16x16x64_bf16 v[32:35], v[20:23], v[136:143], v4\n" \
  "v_smfmac_f32_16x16x64_bf16 v[36:39], v[8:11], v[96:103], v4\n" \
  "v_smfmac_f32_16x16x64_bf16 v[36:39], v[12:15], v[104:111], v4\n" \
  "v_smfmac_f32_16x16x64_bf16 v[36:39], v[16:19], v[160:167], v4\n" \
  "v_smfmac_f32_16x16x64_bf16 v[36:39], v[20:23], v[168:175], v4\n" \
  "s_nop 3\n" \
  "v_add_f32 v32, v32, v34\n" \
  "v_add_f32 v33, v33, v35\n" \
  "s_nop 0\n" \
  "v_add_f32_dpp v48, v32, v33 quad_perm:[1,0,3,2] row_mask:0x5 bank_mask:0xf\n" \
  "v_add_f32 v36, v36, v38\n" \
  "v_add_f32 v37, v37, v39\n" \
  "s_nop 0\n" \
  "v_add_f32_dpp v48, v36, v37 quad_perm:[1,0,3,2] row_mask:0xa bank_mask:0xf\n" \
  "v_cndmask_b32_e64 v48, v5, v48, s[62:63]\n" \
  "s_nop 1\n" \
  "v_max_f32_dpp v54, v48, v48 row_ror:2 row_mask:0xf bank_mask:0xf\n" \
  "s_nop 1\n" \
  "v_max_f32_dpp v54, v54, v54 row_ror:4 row_mask:0xf bank_mask:0xf\n" \
  "s_nop 1\n" \
  "v_max_f32_dpp v54, v54, v54 row_ror:8 row_mask:0xf bank_mask:0xf\n" \
  "s_nop 1\n" \
  "v_max_f32_dpp v54, v54, v54 row_bcast:15 row_mask:0xa bank_mask:0xf\n" \
  "s_nop 1\n" \
  "v_max_f32_dpp v54, v54, v54 row_bcast:31 row_mask:0xc bank_mask:0xf\n" \
  "s_nop 0\n" \
  "v_readlane_b32 s64, v54, 63\n" \
  "s_nop 1\n" \
  "v_max_f32 v54, s64, v6\n" \
  "v_sub_f32 v48, v48, v54\n" \
  "v_sub_f32 v46, v6, v54\n" \
  "v_exp_f32 v48, v48\n" \
  "v_exp_f32 v46, v46\n" \
  "v_mov_b32 v6, v54\n" \
  "v_cvt_pk_bf16_f32 v48, v48, 0\n" \
  "v_mul_f32 v7, v7, v46\n" \
  "v_dot2_f32_bf16 v7, v48, s65, v7\n" \
  "ds_bpermute_b32 v48, v53, v48\n" \
  "v_pk_mul_f32 v[24:25], v[24:25], v[46:47] op_sel_hi:[1,0]\n" \
  "v_pk_mul_f32 v[26:27], v[26:27], v[46:47] op_sel_hi:[1,0]\n" \
  "v_pk_mul_f32 v[28:29], v[28:29], v[46:47] op_sel_hi:[1,0]\n" \
  "v_pk_mul_f32 v[30:31], v[30:31], v[46:47] op_sel_hi:[1,0]\n" \
  "s_waitcnt lgkmcnt(0)\n" \
  "v_mov_b64 v[32:33], 0\n" \
  "v_mov_b64 v[34:35], 0\n" \
  "v_mov_b32_dpp v56, v48 row_newbcast:1 row_mask:0xf bank_mask:0x1\n" \
  "v_mov_b32_dpp v57, v48 row_newbcast:1 row_mask:0xf bank_mask:0x2\n" \
  "v_mov_b32_dpp v58, v48 row_newbcast:3 row_mask:0xf bank_mask:0x1\n" \
  "v_mov_b32_dpp v59, v48 row_newbcast:3 row_mask:0xf bank_mask:0x2\n" \
  "v_mov_b32_dpp v60, v48 row_newbcast:5 row_mask:0xf bank_mask:0x1\n" \
  "v_mov_b32_dpp v61, v48 row_newbcast:5 row_mask:0xf bank_mask:0x2\n" \
  "v_mov_b32_dpp v62, v48 row_newbcast:7 row_mask:0xf bank_mask:0x1\n" \
  "v_mov_b32_dpp v63, v48 row_newbcast:7 row_mask:0xf bank_mask:0x2\n" \
  "v_smfmac_f32_16x16x64_bf16 v[24:27], v[56:59], v[80:87], v4 abid:1\n" \
  "v_mov_b32_dpp v32, v48 row_newbcast:9 row_mask:0xf bank_mask:0x1\n" \
  "v_mov_b32_dpp v33, v48 row_newbcast:9 row_mask:0xf bank_mask:0x2\n" \
  "v_smfmac_f32_16x16x64_bf16 v[28:31], v[56:59], v[88:95], v4 abid:1\n" \
  "v_mov_b32_dpp v34, v48 row_newbcast:11 row_mask:0xf bank_mask:0x1\n" \
  "v_mov_b32_dpp v35, v48 row_newbcast:11 row_mask:0xf bank_mask:0x2\n" \
  "v_smfmac_f32_16x16x64_bf16 v[24:27], v[60:63], v[112:119], v4 abid:1\n" \
  "v_mov_b32_dpp v56, v48 row_newbcast:13 row_mask:0xf bank_mask:0x1\n" \
  "v_mov_b32_dpp v57, v48 row_newbcast:13 row_mask:0xf bank_mask:0x2\n" \
  "v_smfmac_f32_16x16x64_bf16 v[28:31], v[60:63], v[120:127], v4 abid:1\n" \
  "v_mov_b32_dpp v58, v48 row_newbcast:15 row_mask:0xf bank_mask:0x1\n" \
  "v_mov_b32_dpp v59, v48 row_newbcast:15 row_mask:0xf bank_mask:0x2\n" \
  "v_smfmac_f32_16x16x64_bf16 v[24:27], v[32:35], v[144:151], v4 abid:1\n" \
  "v_smfmac_f32_16x16x64_bf16 v[28:31], v[32:35], v[152:159], v4 abid:1\n" \
  "v_smfmac_f32_16x16x64_bf16 v[24:27], v[56:59], v[176:183], v4 abid:1\n" \
  "v_smfmac_f32_16x16x64_bf16 v[28:31], v[56:59], v[184:191], v4 abid:1\n" \
  "s_nop 7\n" \
  "s_setpc_b64 s[66:67]\n" \
  "LB1_%=:\n" \
  "s_nop 1\n" \
  "v_lshlrev_b32 v55, 2, v0\n" \
  "v_add_u32 v55, s73, v55\n" \
  "v_mov_b64 v[32:33], 0\n" \
  "v_mov_b64 v[34:35], 0\n" \
  "v_mov_b64 v[36:37], 0\n" \
  "v_mov_b64 v[38:39], 0\n" \
  "v_smfmac_f32_16x16x64_bf16 v[32:35], v[8:11], a[128:135], v4\n" \
  "v_smfmac_f32_16x16x64_bf16 v[32:35], v[12:15], a[136:143], v4\n" \
  "ds_read_b128 a[128:131], v55 offset:0\n" \
  "ds_read_b128 a[132:135], v55 offset:1024\n" \
  "ds_read_b128 a[136:139], v55 offset:2048\n" \
  "ds_read_b128 a[140:143], v55 offset:3072\n" \
  "v_smfmac_f32_16x16x64_bf16 v[32:35], v[16:19], a[192:199], v4\n" \
  "v_mov_b64 v[40:41], 0\n" \
  "v_smfmac_f32_16x16x64_bf16 v[32:35], v[20:23], a[200:207], v4\n" \
  "v_mov_b64 v[42:43], 0\n" \
  "ds_read_b128 a[192:195], v3 offset:20480\n" \
  "ds_read_b128 a[196:199], v3 offset:21504\n" \
  "ds_read_b128 a[200:203], v3 offset:22528\n" \
  "ds_read_b128 a[204:207], v3 offset:23552\n" \
  "v_smfmac_f32_16x16x64_bf16 v[36:39], v[8:11], a[160:167], v4\n" \
  "v_mov_b64 v[44:45], 0\n" \
  "v_smfmac_f32_16x16x64_bf16 v[36:39], v[12:15], a[168:175], v4\n" \
  "v_mov_b64 v[46:47], 0\n" \
  "ds_read_b128 a[160:163], v3 offset:12288\n" \
  "ds_read_b128 a[164:167], v3 offset:13312\n" \
  "ds_read_b128 a[168:171], v3 offset:14336\n" \
  "ds_read_b128 a[172:175], v3 offset:15360\n" \
  "v_smfmac_f32_16x16x64_bf16 v[36:39], v[16:19], a[224:231], v4\n" \
  "v_smfmac_f32_16x16x64_bf16 v[36:39], v[20:23], a[232:239], v4\n" \
  "s_cmp_lg_u32 s76, 0\n" \
  "s_cbranch_scc1 LB1_1_%=\n" \
  "s_add_u32 s60, s53, 7680\n" \
  "buffer_load_dwordx4 a[224:227], v1, s[40:43], s60 offen offset:0 nt\n" \
  "s_add_u32 s60, s53, 7680\n" \
  "buffer_load_dwordx4 a[228:231], v1, s[40:43], s60 offen offset:128 nt\n" \
  "s_add_u32 s60, s53, 7680\n" \
  "buffer_load_dwordx4 a[232:235], v1, s[40:43], s60 offen offset:256 nt\n" \
  "s_add_u32 s60, s53, 7680\n" \
  "buffer_load_dwordx4 a[236:239], v1, s[40:43], s60 offen offset:384 nt\n" \
  "LB1_1_%=:\n" \
  "v_smfmac_f32_16x16x64_bf16 v[40:43], v[8:11], a[0:7], v4\n" \
  "v_add_f32 v32, v32, v34\n" \
  "v_smfmac_f32_16x16x64_bf16 v[40:43], v[12:15], a[8:15], v4\n" \
  "v_add_f32 v33, v33, v35\n" \
  "ds_read_b128 a[0:3], v3 offset:0\n" \
  "ds_read_b128 a[4:7], v3 offset:1024\n" \
  "ds_read_b128 a[8:11], v3 offset:2048\n" \
  "ds_read_b128 a[12:15], v3 offset:3072\n" \
  "v_smfmac_f32_16x16x64_bf16 v[40:43], v[16:19], a[64:71], v4\n" \
  "v_add_f32_dpp v49, v32, v33 quad_perm:[1,0,3,2] row_mask:0x5 bank_mask:0xf\n" \
  "v_smfmac_f32_16x16x64_bf16 v[40:43], v[20:23], a[72:79], v4\n" \
  "v_add_f32 v36, v36, v38\n" \
  "ds_read_b128 a[64:67], v3 offset:16384\n" \
  "ds_read_b128 a[68:71], v3 offset:17408\n" \
  "ds_read_b128 a[72:75], v3 offset:18432\n" \
  "ds_read_b128 a[76:79], v3 offset:19456\n" \
  "v_smfmac_f32_16x16x64_bf16 v[44:47], v[8:11], a[32:39], v4\n" \
  "v_add_f32 v37, v37, v39\n" \
  "v_smfmac_f32_16x16x64_bf16 v[44:47], v[12:15], a[40:47], v4\n" \
  "v_add_f32_dpp v49, v36, v37 quad_perm:[1,0,3,2] row_mask:0xa bank_mask:0xf\n" \
  "ds_read_b128 a[32:35], v3 offset:8192\n" \
  "ds_read_b128 a[36:39], v3 offset:9216\n" \
  "ds_read_b128 a[40:43], v3 offset:10240\n" \
  "ds_read_b128 a[44:47], v3 offset:11264\n" \
  "v_smfmac_f32_16x16x64_bf16 v[44:47], v[16:19], a[96:103], v4\n" \
  "v_mov_b64 v[32:33], 0\n" \
  "v_smfmac_f32_16x16x64_bf16 v[44:47], v[20:23], a[104:111], v4\n" \
  "v_mov_b64 v[34:35], 0\n" \
  "ds_read_b128 a[96:99], v3 offset:24576\n" \
  "ds_read_b128 a[100:103], v3 offset:25600\n" \
  "ds_read_b128 a[104:107], v3 offset:26624\n" \
  "ds_read_b128 a[108:111], v3 offset:27648\n" \
  "v_mov_b64 v[36:37], 0\n" \
  "v_mov_b64 v[38:39], 0\n" \
  "v_smfmac_f32_16x16x64_bf16 v[32:35], v[8:11], v[64:71], v4\n" \
  "v_add_f32 v40, v40, v42\n" \
  "v_smfmac_f32_16x16x64_bf16 v[32:35], v[12:15], v[72:79], v4\n" \
  "v_add_f32 v41, v41, v43\n" \
  "ds_read_b128 v[64:67], v3 offset:28672\n" \
  "ds_read_b128 v[68:71], v3 offset:29696\n" \
  "ds_read_b128 v[72:75], v3 offset:30720\n" \
  "ds_read_b128 v[76:79], v3 offset:31744\n" \
  "v_smfmac_f32_16x16x64_bf16 v[32:35], v[16:19], v[128:135], v4\n" \
  "v_add_f32_dpp v48, v40, v41 quad_perm:[1,0,3,2] row_mask:0x5 bank_mask:0xf\n" \
  "v_smfmac_f32_16x16x64_bf16 v[32:35], v[20:23], v[136:143], v4\n" \
  "v_add_f32 v44, v44, v46\n" \
  "ds_read_b128 v[128:131], v3 offset:4096\n" \
  "ds_read_b128 v[132:135], v3 offset:5120\n" \
  "ds_read_b128 v[136:139], v3 offset:6144\n" \
  "ds_read_b128 v[140:143], v3 offset:7168\n" \
  "v_smfmac_f32_16x16x64_bf16 v[36:39], v[8:11], v[96:103], v4\n" \
  "v_add_f32 v45, v45, v47\n" \
  "v_smfmac_f32_16x16x64_bf16 v[36:39], v[12:15], v[104:111], v4\n" \
  "v_add_f32_dpp v48, v44, v45 quad_perm:[1,0,3,2] row_mask:0xa bank_mask:0xf\n" \
  "ds_read_b128 v[96:99], v55 offset:4096\n" \
  "ds_read_b128 v[100:103], v55 offset:5120\n" \
  "ds_read_b128 v[104:107], v55 offset:6144\n" \
  "ds_read_b128 v[108:111], v55 offset:7168\n" \
  "v_smfmac_f32_16x16x64_bf16 v[36:39], v[16:19], v[160:167], v4\n" \
  "v_mov_b64 v[40:41], 0\n" \
  "v_smfmac_f32_16x16x64_bf16 v[36:39], v[20:23], v[168:175], v4\n" \
  "v_mov_b64 v[42:43], 0\n" \
  "v_mov_b64 v[44:45], 0\n" \
  "v_mov_b64 v[46:47], 0\n" \
  "s_waitcnt lgkmcnt(15)\n" \
  "v_smfmac_f32_16x16x64_bf16 v[40:43], v[8:11], a[0:7], v4\n" \
  "v_add_f32 v32, v32, v34\n" \
  "v_smfmac_f32_16x16x64_bf16 v[40:43], v[12:15], a[8:15], v4\n" \
  "v_add_f32 v33, v33, v35\n" \
  "v_mov_b64 v[160:161], v[24:25]\n" \
  "v_mov_b64 v[162:163], v[26:27]\n" \
  "v_mov_b64 v[164:165], v[28:29]\n" \
  "v_mov_b64 v[166:167], v[30:31]\n" \
  "v_mov_b32 v168, v6\n" \
  "v_mov_b32 v169, v7\n" \
  "s_waitcnt lgkmcnt(15)\n" \
  "v_smfmac_f32_16x16x64_bf16 v[40:43], v[16:19], a[64:71], v4\n" \
  "v_add_f32_dpp v50, v32, v33 quad_perm:[1,0,3,2] row_mask:0x5 bank_mask:0xf\n" \
  "v_smfmac_f32_16x16x64_bf16 v[40:43], v[20:23], a[72:79], v4\n" \
  "v_add_f32 v36, v36, v38\n" \
  "s_waitcnt lgkmcnt(15)\n" \
  "v_smfmac_f32_16x16x64_bf16 v[44:47], v[8:11], a[32:39], v4\n" \
  "v_add_f32 v37, v37, v39\n" \
  "v_smfmac_f32_16x16x64_bf16 v[44:47], v[12:15], a[40:47], v4\n" \
  "v_add_f32_dpp v50, v36, v37 quad_perm:[1,0,3,2] row_mask:0xa bank_mask:0xf\n" \
  "s_waitcnt lgkmcnt(12)\n" \
  "v_smfmac_f32_16x16x64_bf16 v[44:47], v[16:19], a[96:103], v4\n" \
  "v_mov_b64 v[32:33], 0\n" \
  "v_smfmac_f32_16x16x64_bf16 v[44:47], v[20:23], a[104:111], v4\n" \
  "v_mov_b64 v[34:35], 0\n" \
  "v_mov_b64 v[36:37], 0\n" \
  "v_mov_b64 v[38:39], 0\n" \
  "s_cmp_eq_u32 s56, 0\n" \
  "s_cbranch_scc1 LB1_2_%=\n" \
  "v_smfmac_f32_16x16x64_bf16 v[32:35], v[8:11], a[128:135], v4\n" \
  "v_smfmac_f32_16x16x64_bf16 v[32:35], v[12:15], a[136:143], v4\n" \
  "LB1_2_%=:\n" \
  "v_smfmac_f32_16x16x64_bf16 v[32:35], v[16:19], v[224:231], v4\n" \
  "v_add_f32 v40, v40, v42\n" \
  "v_smfmac_f32_16x16x64_bf16 v[32:35], v[20:23], v[232:239], v4\n" \
  "v_add_f32 v41, v41, v43\n" \
  "v_smfmac_f32_16x16x64_bf16 v[36:39], v[8:11], v[192:199], v4\n" \
  "v_add_f32_dpp v51, v40, v41 quad_perm:[1,0,3,2] row_mask:0x5 bank_mask:0xf\n" \
  "v_smfmac_f32_16x16x64_bf16 v[36:39], v[12:15], v[200:207], v4\n" \
  "v_add_f32 v44, v44, v46\n" \
  "v_add_f32 v45, v45, v47\n" \
  "s_nop 0\n" \
  "v_add_f32_dpp v51, v44, v45 quad_perm:[1,0,3,2] row_mask:0xa bank_mask:0xf\n" \
  "v_add_f32 v32, v32, v34\n" \
  "v_add_f32 v33, v33, v35\n" \
  "s_nop 0\n" \
  "v_add_f32_dpp v52, v32, v33 quad_perm:[1,0,3,2] row_mask:0x5 bank_mask:0xf\n" \
  "v_add_f32 v36, v36, v38\n" \
  "v_add_f32 v37, v37, v39\n" \
  "s_nop 0\n" \
  "v_add_f32_dpp v52, v36, v37 quad_perm:[1,0,3,2] row_mask:0xa bank_mask:0xf\n" \
  "v_cndmask_b32_e64 v48, v5, v48, s[80:81]\n" \
  "v_cndmask_b32_e64 v49, v5, v49, s[80:81]\n" \
  "v_cndmask_b32_e64 v50, v5, v50, s[80:81]\n" \
  "v_cndmask_b32_e64 v51, v5, v51, s[80:81]\n" \
  "v_cndmask_b32_e64 v52, v5, v52, s[78:79]\n" \
  "v_max3_f32 v54, v48, v49, v50\n" \
  "v_max3_f32 v54, v54, v51, v52\n" \
  "s_nop 1\n" \
  "v_max_f32_dpp v54, v54, v54 row_ror:2 row_mask:0xf bank_mask:0xf\n" \
  "s_nop 1\n" \
  "v_max_f32_dpp v54, v54, v54 row_ror:4 row_mask:0xf bank_mask:0xf\n" \
  "s_nop 1\n" \
  "v_max_f32_dpp v54, v54, v54 row_ror:8 row_mask:0xf bank_mask:0xf\n" \
  "s_nop 1\n" \
  "v_max_f32_dpp v54, v54, v54 row_bcast:15 row_mask:0xa bank_mask:0xf\n" \
  "s_nop 1\n" \
  "v_max_f32_dpp v54, v54, v54 row_bcast:31 row_mask:0xc bank_mask:0xf\n" \
  "s_nop 0\n" \
  "v_readlane_b32 s64, v54, 63\n" \
  "s_nop 1\n" \
  "v_mov_b32 v6, s64\n" \
  "v_subrev_f32 v48, s64, v48\n" \
  "v_subrev_f32 v49, s64, v49\n" \
  "v_subrev_f32 v50, s64, v50\n" \
  "v_subrev_f32 v51, s64, v51\n" \
  "v_subrev_f32 v52, s64, v52\n" \
  "v_exp_f32 v48, v48\n" \
  "v_exp_f32 v49, v49\n" \
  "v_exp_f32 v50, v50\n" \
  "v_exp_f32 v51, v51\n" \
  "v_exp_f32 v52, v52\n" \
  "v_cvt_pk_bf16_f32 v48, v48, 0\n" \
  "v_cvt_pk_bf16_f32 v49, v49, 0\n" \
  "v_cvt_pk_bf16_f32 v50, v50, 0\n" \
  "v_cvt_pk_bf16_f32 v51, v51, 0\n" \
  "v_cvt_pk_bf16_f32 v52, v52, 0\n" \
  "v_dot2_f32_bf16 v7, v48, s65, 0\n" \
  "ds_bpermute_b32 v48, v53, v48\n" \
  "v_dot2_f32_bf16 v7, v49, s65, v7\n" \
  "ds_bpermute_b32 v49, v53, v49\n" \
  "v_dot2_f32_bf16 v7, v50, s65, v7\n" \
  "ds_bpermute_b32 v50, v53, v50\n" \
  "v_dot2_f32_bf16 v7, v51, s65, v7\n" \
  "ds_bpermute_b32 v51, v53, v51\n" \
  "v_dot2_f32_bf16 v7, v52, s65, v7\n" \
  "ds_bpermute_b32 v52, v53, v52\n" \
  "s_waitcnt lgkmcnt(0)\n" \
  "s_nop 0\n" \
  "s_setpc_b64 s[66:67]\n" \
  "LB3a_%=:\n" \
  "s_nop 1\n" \
  "v_mov_b64 v[24:25], 0\n" \
  "v_mov_b64 v[26:27], 0\n" \
  "v_mov_b64 v[28:29], 0\n" \
  "v_mov_b64 v[30:31], 0\n" \
  "v_mov_b64 v[32:33], 0\n" \
  "v_mov_b64 v[34:35], 0\n" \
  "v_mov_b32_dpp v56, v48 row_newbcast:1 row_mask:0xf bank_mask:0x1\n" \
  "v_mov_b32_dpp v57, v48 row_newbcast:1 row_mask:0xf bank_mask:0x2\n" \
  "v_mov_b32_dpp v58, v48 row_newbcast:3 row_mask:0xf bank_mask:0x1\n" \
  "v_mov_b32_dpp v59, v48 row_newbcast:3 row_mask:0xf bank_mask:0x2\n" \
  "v_mov_b32_dpp v60, v48 row_newbcast:5 row_mask:0xf bank_mask:0x1\n" \
  "v_mov_b32_dpp v61, v48 row_newbcast:5 row_mask:0xf bank_mask:0x2\n" \
  "v_mov_b32_dpp v62, v48 row_newbcast:7 row_mask:0xf bank_mask:0x1\n" \
  "v_mov_b32_dpp v63, v48 row_newbcast:7 row_mask:0xf bank_mask:0x2\n" \
  "v_smfmac_f32_16x16x64_bf16 v[24:27], v[56:59], a[16:23], v4 abid:1\n" \
  "v_mov_b32_dpp v32, v48 row_newbcast:9 row_mask:0xf bank_mask:0x1\n" \
  "v_mov_b32_dpp v33, v48 row_newbcast:9 row_mask:0xf bank_mask:0x2\n" \
  "v_smfmac_f32_16x16x64_bf16 v[28:31], v[56:59], a[24:31], v4 abid:1\n" \
  "v_mov_b32_dpp v34, v48 row_newbcast:11 row_mask:0xf bank_mask:0x1\n" \
  "v_mov_b32_dpp v35, v48 row_newbcast:11 row_mask:0xf bank_mask:0x2\n" \
  "v_smfmac_f32_16x16x64_bf16 v[24:27], v[60:63], a[48:55], v4 abid:1\n" \
  "v_mov_b32_dpp v56, v48 row_newbcast:13 row_mask:0xf bank_mask:0x1\n" \
  "v_mov_b32_dpp v57, v48 row_newbcast:13 row_mask:0xf bank_mask:0x2\n" \
  "v_smfmac_f32_16x16x64_bf16 v[28:31], v[60:63], a[56:63], v4 abid:1\n" \
  "v_mov_b32_dpp v58, v48 row_newbcast:15 row_mask:0xf bank_mask:0x1\n" \
  "v_mov_b32_dpp v59, v48 row_newbcast:15 row_mask:0xf bank_mask:0x2\n" \
  "v_smfmac_f32_16x16x64_bf16 v[24:27], v[32:35], a[80:87], v4 abid:1\n" \
  "v_smfmac_f32_16x16x64_bf16 v[28:31], v[32:35], a[88:95], v4 abid:1\n" \
  "v_smfmac_f32_16x16x64_bf16 v[24:27], v[56:59], a[112:119], v4 abid:1\n" \
  "v_smfmac_f32_16x16x64_bf16 v[28:31], v[56:59], a[120:127], v4 abid:1\n" \
  "s_nop 7\n" \
  "s_setpc_b64 s[66:67]\n" \
  "LB3b_%=:\n" \
  "s_nop 1\n" \
  "v_mov_b64 v[32:33], 0\n" \
  "v_mov_b64 v[34:35], 0\n" \
  "v_mov_b32_dpp v56, v49 row_newbcast:1 row_mask:0xf bank_mask:0x1\n" \
  "v_mov_b32_dpp v57, v49 row_newbcast:1 row_mask:0xf bank_mask:0x2\n" \
  "v_mov_b32_dpp v58, v49 row_newbcast:3 row_mask:0xf bank_mask:0x1\n" \
  "v_mov_b32_dpp v59, v49 row_newbcast:3 row_mask:0xf bank_mask:0x2\n" \
  "v_mov_b32_dpp v60, v49 row_newbcast:5 row_mask:0xf bank_mask:0x1\n" \
  "v_mov_b32_dpp v61, v49 row_newbcast:5 row_mask:0xf bank_mask:0x2\n" \
  "v_mov_b32_dpp v62, v49 row_newbcast:7 row_mask:0xf bank_mask:0x1\n" \
  "v_mov_b32_dpp v63, v49 row_newbcast:7 row_mask:0xf bank_mask:0x2\n" \
  "v_smfmac_f32_16x16x64_bf16 v[24:27], v[56:59], a[144:151], v4 abid:1\n" \
  "v_mov_b32_dpp v32, v49 row_newbcast:9 row_mask:0xf bank_mask:0x1\n" \
  "v_mov_b32_dpp v33, v49 row_newbcast:9 row_mask:0xf bank_mask:0x2\n" \
  "v_smfmac_f32_16x16x64_bf16 v[28:31], v[56:59], a[152:159], v4 abid:1\n" \
  "v_mov_b32_dpp v34, v49 row_newbcast:11 row_mask:0xf bank_mask:0x1\n" \
  "v_mov_b32_dpp v35, v49 row_newbcast:11 row_mask:0xf bank_mask:0x2\n" \
  "v_smfmac_f32_16x16x64_bf16 v[24:27], v[60:63], a[176:183], v4 abid:1\n" \
  "v_mov_b32_dpp v56, v49 row_newbcast:13 row_mask:0xf bank_mask:0x1\n" \
  "v_mov_b32_dpp v57, v49 row_newbcast:13 row_mask:0xf bank_mask:0x2\n" \
  "v_smfmac_f32_16x16x64_bf16 v[28:31], v[60:63], a[184:191], v4 abid:1\n" \
  "v_mov_b32_dpp v58, v49 row_newbcast:15 row_mask:0xf bank_mask:0x1\n" \
  "v_mov_b32_dpp v59, v49 row_newbcast:15 row_mask:0xf bank_mask:0x2\n" \
  "v_smfmac_f32_16x16x64_bf16 v[24:27], v[32:35], a[208:215], v4 abid:1\n" \
  "v_mov_b32_dpp v60, v50 row_newbcast:1 row_mask:0xf bank_mask:0x1\n" \
  "v_mov_b32_dpp v61, v50 row_newbcast:1 row_mask:0xf bank_mask:0x2\n" \
  "v_smfmac_f32_16x16x64_bf16 v[28:31], v[32:35], a[216:223], v4 abid:1\n" \
  "v_mov_b32_dpp v62, v50 row_newbcast:3 row_mask:0xf bank_mask:0x1\n" \
  "v_mov_b32_dpp v63, v50 row_newbcast:3 row_mask:0xf bank_mask:0x2\n" \
  "v_smfmac_f32_16x16x64_bf16 v[24:27], v[56:59], a[240:247], v4 abid:1\n" \
  "v_mov_b32_dpp v32, v50 row_newbcast:5 row_mask:0xf bank_mask:0x1\n" \
  "v_mov_b32_dpp v33, v50 row_newbcast:5 row_mask:0xf bank_mask:0x2\n" \
  "v_smfmac_f32_16x16x64_bf16 v[28:31], v[56:59], a[248:255], v4 abid:1\n" \
  "v_mov_b32_dpp v34, v50 row_newbcast:7 row_mask:0xf bank_mask:0x1\n" \
  "v_mov_b32_dpp v35, v50 row_newbcast:7 row_mask:0xf bank_mask:0x2\n" \
  "v_smfmac_f32_16x16x64_bf16 v[24:27], v[60:63], v[80:87], v4 abid:1\n" \
  "v_mov_b32_dpp v56, v50 row_newbcast:9 row_mask:0xf bank_mask:0x1\n" \
  "v_mov_b32_dpp v57, v50 row_newbcast:9 row_mask:0xf bank_mask:0x2\n" \
  "v_smfmac_f32_16x16x64_bf16 v[28:31], v[60:63], v[88:95], v4 abid:1\n" \
  "v_mov_b32_dpp v58, v50 row_newbcast:11 row_mask:0xf bank_mask:0x1\n" \
  "v_mov_b32_dpp v59, v50 row_newbcast:11 row_mask:0xf bank_mask:0x2\n" \
  "v_smfmac_f32_16x16x64_bf16 v[24:27], v[32:35], v[112:119], v4 abid:1\n" \
  "v_mov_b32_dpp v60, v50 row_newbcast:13 row_mask:0xf bank_mask:0x1\n" \
  "v_mov_b32_dpp v61, v50 row_newbcast:13 row_mask:0xf bank_mask:0x2\n" \
  "v_smfmac_f32_16x16x64_bf16 v[28:31], v[32:35], v[120:127], v4 abid:1\n" \
  "v_mov_b32_dpp v62, v50 row_newbcast:15 row_mask:0xf bank_mask:0x1\n" \
  "v_mov_b32_dpp v63, v50 row_newbcast:15 row_mask:0xf bank_mask:0x2\n" \
  "v_smfmac_f32_16x16x64_bf16 v[24:27], v[56:59], v[144:151], v4 abid:1\n" \
  "v_mov_b32_dpp v32, v51 row_newbcast:1 row_mask:0xf bank_mask:0x1\n" \
  "v_mov_b32_dpp v33, v51 row_newbcast:1 row_mask:0xf bank_mask:0x2\n" \
  "v_smfmac_f32_16x16x64_bf16 v[28:31], v[56:59], v[152:159], v4 abid:1\n" \
  "v_mov_b32_dpp v34, v51 row_newbcast:3 row_mask:0xf bank_mask:0x1\n" \
  "v_mov_b32_dpp v35, v51 row_newbcast:3 row_mask:0xf bank_mask:0x2\n" \
  "v_smfmac_f32_16x16x64_bf16 v[24:27], v[60:63], v[176:183], v4 abid:1\n" \
  "v_mov_b32_dpp v56, v51 row_newbcast:5 row_mask:0xf bank_mask:0x1\n" \
  "v_mov_b32_dpp v57, v51 row_newbcast:5 row_mask:0xf bank_mask:0x2\n" \
  "v_smfmac_f32_16x16x64_bf16 v[28:31], v[60:63], v[184:191], v4 abid:1\n" \
  "v_mov_b32_dpp v58, v51 row_newbcast:7 row_mask:0xf bank_mask:0x1\n" \
  "v_mov_b32_dpp v59, v51 row_newbcast:7 row_mask:0xf bank_mask:0x2\n" \
  "v_smfmac_f32_16x16x64_bf16 v[24:27], v[32:35], v[128:135], v4 abid:1\n" \
  "v_mov_b32_dpp v60, v51 row_newbcast:9 row_mask:0xf bank_mask:0x1\n" \
  "v_mov_b32_dpp v61, v51 row_newbcast:9 row_mask:0xf bank_mask:0x2\n" \
  "v_smfmac_f32_16x16x64_bf16 v[28:31], v[32:35], v[136:143], v4 abid:1\n" \
  "v_mov_b32_dpp v62, v51 row_newbcast:11 row_mask:0xf bank_mask:0x1\n" \
  "v_mov_b32_dpp v63, v51 row_newbcast:11 row_mask:0xf bank_mask:0x2\n" \
  "v_smfmac_f32_16x16x64_bf16 v[24:27], v[56:59], a[160:167], v4 abid:1\n" \
  "v_mov_b32_dpp v32, v51 row_newbcast:13 row_mask:0xf bank_mask:0x1\n" \
  "v_mov_b32_dpp v33, v51 row_newbcast:13 row_mask:0xf bank_mask:0x2\n" \
  "v_smfmac_f32_16x16x64_bf16 v[28:31], v[56:59], a[168:175], v4 abid:1\n" \
  "v_mov_b32_dpp v34, v51 row_newbcast:15 row_mask:0xf bank_mask:0x1\n" \
  "v_mov_b32_dpp v35, v51 row_newbcast:15 row_mask:0xf bank_mask:0x2\n" \
  "v_smfmac_f32_16x16x64_bf16 v[24:27], v[60:63], a[192:199], v4 abid:1\n" \
  "v_smfmac_f32_16x16x64_bf16 v[28:31], v[60:63], a[200:207], v4 abid:1\n" \
  "v_smfmac_f32_16x16x64_bf16 v[24:27], v[32:35], v[64:71], v4 abid:1\n" \
  "v_smfmac_f32_16x16x64_bf16 v[28:31], v[32:35], v[72:79], v4 abid:1\n" \
  "s_cmp_eq_u32 s56, 0\n" \
  "s_cbranch_scc1 LB3b_1_%=\n" \
  "v_mov_b32_dpp v56, v52 row_newbcast:1 row_mask:0xf bank_mask:0x1\n" \
  "v_mov_b32_dpp v57, v52 row_newbcast:1 row_mask:0xf bank_mask:0x2\n" \
  "v_mov_b32_dpp v58, v52 row_newbcast:3 row_mask:0xf bank_mask:0x1\n" \
  "v_mov_b32_dpp v59, v52 row_newbcast:3 row_mask:0xf bank_mask:0x2\n" \
  "s_nop 1\n" \
  "v_smfmac_f32_16x16x64_bf16 v[24:27], v[56:59], v[96:103], v4 abid:1\n" \
  "v_smfmac_f32_16x16x64_bf16 v[28:31], v[56:59], v[104:111], v4 abid:1\n" \
  "LB3b_1_%=:\n" \
  "s_cmp_eq_u32 s76, 0\n" \
  "s_cbranch_scc1 LB3b_2_%=\n" \
  "v_mov_b32_dpp v56, v52 row_newbcast:5 row_mask:0xf bank_mask:0x1\n" \
  "v_mov_b32_dpp v57, v52 row_newbcast:5 row_mask:0xf bank_mask:0x2\n" \
  "v_mov_b32_dpp v58, v52 row_newbcast:7 row_mask:0xf bank_mask:0x1\n" \
  "v_mov_b32_dpp v59, v52 row_newbcast:7 row_mask:0xf bank_mask:0x2\n" \
  "s_nop 1\n" \
  "v_smfmac_f32_16x16x64_bf16 v[24:27], v[56:59], v[208:215], v4 abid:1\n" \
  "v_mov_b32_dpp v60, v52 row_newbcast:9 row_mask:0xf bank_mask:0x1\n" \
  "v_mov_b32_dpp v61, v52 row_newbcast:9 row_mask:0xf bank_mask:0x2\n" \
  "v_smfmac_f32_16x16x64_bf16 v[28:31], v[56:59], v[216:223], v4 abid:1\n" \
  "v_mov_b32_dpp v62, v52 row_newbcast:11 row_mask:0xf bank_mask:0x1\n" \
  "v_mov_b32_dpp v63, v52 row_newbcast:11 row_mask:0xf bank_mask:0x2\n" \
  "s_nop 1\n" \
  "v_smfmac_f32_16x16x64_bf16 v[24:27], v[60:63], v[240:247], v4 abid:1\n" \
  "v_smfmac_f32_16x16x64_bf16 v[28:31], v[60:63], v[248:255], v4 abid:1\n" \
  "LB3b_2_%=:\n" \
  "s_nop 7\n" \
  "s_setpc_b64 s[66:67]\n" \
  "LQL_%=:\n" \
  "s_nop 1\n" \
  "v_and_b32 v54, 0xc0, v0\n" \
  "v_and_b32 v55, 12, v0\n" \
  "v_lshl_add_u32 v54, v55, 2, v54\n" \
  "v_add_u32 v54, s70, v54\n" \
  "s_mov_b32 exec_lo, 0xff00ff\n" \
  "s_mov_b32 exec_hi, 0xff00ff\n" \
  "ds_read_b128 v[8:11], v54 offset:0\n" \
  "ds_read_b128 v[12:15], v54 offset:256\n" \
  "s_mov_b32 exec_lo, 0xff00ff00\n" \
  "s_mov_b32 exec_hi, 0xff00ff00\n" \
  "ds_read_b128 v[16:19], v54 offset:0\n" \
  "ds_read_b128 v[20:23], v54 offset:256\n" \
  "s_mov_b64 exec, -1\n" \
  "s_waitcnt lgkmcnt(0)\n" \
  "v_max3_u32 v55, v8, v9, v10\n" \
  "v_max3_u32 v55, v55, v11, v12\n" \
  "v_max3_u32 v55, v55, v13, v14\n" \
  "v_max3_u32 v55, v55, v15, v16\n" \
  "v_max3_u32 v55, v55, v17, v18\n" \
  "v_max3_u32 v55, v55, v19, v20\n" \
  "v_max3_u32 v55, v55, v21, v22\n" \
  "v_max_u32 v55, v55, v23\n" \
  "v_cmp_eq_u32 vcc, -1, v55\n" \
  "s_cbranch_vccz LQL_2_%=\n" \
  "s_add_u32 s71, s71, s92\n" \
  "s_add_u32 m0, s70, s92\n" \
  "s_mov_b32 exec_hi, 0\n" \
  "buffer_load_dword v0, s[48:51], s71 offen sc1 lds\n" \
  "s_mov_b32 exec_hi, -1\n" \
  "s_sleep 8\n" \
  "s_mov_b32 exec_hi, 0\n" \
  "buffer_load_dword v0, s[48:51], s71 offen sc1 lds\n" \
  "s_mov_b32 exec_hi, -1\n" \
  "LQL_1_%=:\n" \
  "s_waitcnt vmcnt(1)\n" \
  "s_mov_b32 exec_lo, 0xff00ff\n" \
  "s_mov_b32 exec_hi, 0xff00ff\n" \
  "ds_read_b128 v[8:11], v54 offset:0\n" \
  "ds_read_b128 v[12:15], v54 offset:256\n" \
  "s_mov_b32 exec_lo, 0xff00ff00\n" \
  "s_mov_b32 exec_hi, 0xff00ff00\n" \
  "ds_read_b128 v[16:19], v54 offset:0\n" \
  "ds_read_b128 v[20:23], v54 offset:256\n" \
  "s_mov_b64 exec, -1\n" \
  "s_waitcnt lgkmcnt(0)\n" \
  "v_max3_u32 v55, v8, v9, v10\n" \
  "v_max3_u32 v55, v55, v11, v12\n" \
  "v_max3_u32 v55, v55, v13, v14\n" \
  "v_max3_u32 v55, v55, v15, v16\n" \
  "v_max3_u32 v55, v55, v17, v18\n" \
  "v_max3_u32 v55, v55, v19, v20\n" \
  "v_max3_u32 v55, v55, v21, v22\n" \
  "v_max_u32 v55, v55, v23\n" \
  "v_cmp_eq_u32 vcc, -1, v55\n" \
  "s_cbranch_vccz LQL_2_%=\n" \
  "s_sub_u32 s68, s68, 1\n" \
  "s_cmp_lt_i32 s68, 0\n" \
  "s_cbranch_scc1 LQL_2_%=\n" \
  "s_mov_b32 exec_hi, 0\n" \
  "buffer_load_dword v0, s[48:51], s71 offen sc1 lds\n" \
  "s_mov_b32 exec_hi, -1\n" \
  "s_branch LQL_1_%=\n" \
  "LQL_2_%=:\n" \
  "s_nop 1\n" \
  "s_setpc_b64 s[66:67]\n" \
  "LDU_%=:\n" \
  "s_nop 1\n" \
  "v_and_b32 v54, 60, v0\n" \
  "v_lshlrev_b32 v54, 3, v54\n" \
  "v_add_f32_dpp v7, v7, v7 row_ror:2 row_mask:0xf bank_mask:0xf\n" \
  "v_and_b32 v55, 64, v0\n" \
  "v_lshrrev_b32 v55, 2, v55\n" \
  "s_add_u32 s61, s71, s83\n" \
  "v_add_f32_dpp v7, v7, v7 row_ror:4 row_mask:0xf bank_mask:0xf\n" \
  "v_add3_u32 v55, v55, v54, s61\n" \
  "s_mov_b32 exec_hi, 0\n" \
  "ds_write_b128 v55, v[24:27] offset:0\n" \
  "ds_write_b128 v55, v[28:31] offset:512\n" \
  "s_mov_b32 exec_hi, -1\n" \
  "v_add_f32_dpp v7, v7, v7 row_ror:8 row_mask:0xf bank_mask:0xf\n" \
  "v_lshlrev_b32 v54, 2, v0\n" \
  "s_nop 0\n" \
  "v_add_f32_dpp v7, v7, v7 row_bcast:15 row_mask:0xa bank_mask:0xf\n" \
  "s_lshl_b32 s61, s59, 2\n" \
  "s_add_u32 s61, s61, s77\n" \
  "v_mov_b32 v55, s61\n" \
  "v_add_f32_dpp v7, v7, v7 row_bcast:31 row_mask:0xc bank_mask:0xf\n" \
  "s_mov_b32 exec_lo, 0\n" \
  "s_mov_b32 exec_hi, 0x80000000\n" \
  "ds_write_b32 v55, v6 offset:0\n" \
  "ds_write_b32 v55, v7 offset:16\n" \
  "s_mov_b64 exec, -1\n" \
  "s_add_u32 s61, s71, s72\n" \
  "v_add_u32 v55, s61, v54\n" \
  "v_mov_b32 v52, s77\n" \
  "s_waitcnt lgkmcnt(0)\n" \
  "s_barrier\n" \
  "ds_read_b128 v[24:27], v52 offset:0\n" \
  "ds_read_b128 v[28:31], v52 offset:16\n" \
  "ds_read_b128 v[32:35], v55 offset:0\n" \
  "ds_read_b128 v[36:39], v55 offset:1024\n" \
  "ds_read_b128 v[40:43], v55 offset:2048\n" \
  "ds_read_b128 v[44:47], v55 offset:3072\n" \
  "s_waitcnt lgkmcnt(4)\n" \
  "v_max3_f32 v55, v24, v25, v26\n" \
  "v_max_f32 v55, v55, v27\n" \
  "v_sub_f32 v24, v24, v55\n" \
  "v_sub_f32 v25, v25, v55\n" \
  "v_sub_f32 v26, v26, v55\n" \
  "v_sub_f32 v27, v27, v55\n" \
  "v_exp_f32 v24, v24\n" \
  "v_exp_f32 v25, v25\n" \
  "v_exp_f32 v26, v26\n" \
  "v_exp_f32 v27, v27\n" \
  "v_mul_f32 v52, v24, v28\n" \
  "v_fmac_f32 v52, v25, v29\n" \
  "v_fmac_f32 v52, v26, v30\n" \
  "v_fmac_f32 v52, v27, v31\n" \
  "s_waitcnt lgkmcnt(3)\n" \
  "v_mul_f32 v48, v24, v32\n" \
  "v_mul_f32 v49, v24, v33\n" \
  "v_mul_f32 v50, v24, v34\n" \
  "v_mul_f32 v51, v24, v35\n" \
  "s_waitcnt lgkmcnt(2)\n" \
  "v_fmac_f32 v48, v25, v36\n" \
  "v_fmac_f32 v49, v25, v37\n" \
  "v_fmac_f32 v50, v25, v38\n" \
  "v_fmac_f32 v51, v25, v39\n" \
  "s_waitcnt lgkmcnt(1)\n" \
  "v_fmac_f32 v48, v26, v40\n" \
  "v_fmac_f32 v49, v26, v41\n" \
  "v_fmac_f32 v50, v26, v42\n" \
  "v_fmac_f32 v51, v26, v43\n" \
  "s_waitcnt lgkmcnt(0)\n" \
  "v_fmac_f32 v48, v27, v44\n" \
  "v_fmac_f32 v49, v27, v45\n" \
  "v_fmac_f32 v50, v27, v46\n" \
  "v_fmac_f32 v51, v27, v47\n" \
  "s_mov_b32 s74, 0x10000\n" \
  "s_mov_b32 s75, 0\n" \
  "v_cndmask_b32_e64 v48, v48, v55, s[74:75]\n" \
  "v_cndmask_b32_e64 v49, v49, v52, s[74:75]\n" \
  "s_mov_b32 exec_lo, 0x1ffff\n" \
  "s_mov_b32 exec_hi, 0\n" \
  "buffer_store_dwordx4 v[48:51], v54, s[48:51], s70 offen sc1\n" \
  "s_mov_b64 exec, -1\n" \
  "s_nop 1\n" \
  "s_setpc_b64 s[66:67]\n" \
  "LRW_%=:\n" \
  "s_nop 1\n" \
  "LRW_1_%=:\n" \
  "buffer_load_dword v55, off, s[48:51], s89 sc1\n" \
  "s_waitcnt vmcnt(0)\n" \
  "v_readfirstlane_b32 s61, v55\n" \
  "s_nop 3\n" \
  "s_cmp_eq_u32 s61, s88\n" \
  "s_cbranch_scc1 LRW_2_%=\n" \
  "s_sub_u32 s68, s68, 1\n" \
  "s_cmp_lt_i32 s68, 0\n" \
  "s_cbranch_scc1 LRW_2_%=\n" \
  "s_branch LRW_1_%=\n" \
  "LRW_2_%=:\n" \
  "s_nop 1\n" \
  "s_setpc_b64 s[66:67]\n" \
  "Lend_%=:\n" \
  ""
#define ATT_CLOB "v0", "v1", "v2", "v3", "v4", "v5", "v6", "v7", "v8", "v9", "v10", "v11", "v12", "v13", "v14", "v15", "v16", "v17", "v18", "v19", "v20", "v21", "v22", "v23", "v24", "v25", "v26", "v27", "v28", "v29", "v30", "v31", "v32", "v33", "v34", "v35", "v36", "v37", "v38", "v39", "v40", "v41", "v42", "v43", "v44", "v45", "v46", "v47", "v48", "v49", "v50", "v51", "v52", "v53", "v54", "v55", "v56", "v57", "v58", "v59", "v60", "v61", "v62", "v63", "v64", "v65", "v66", "v67", "v68", "v69", "v70", "v71", "v72", "v73", "v74", "v75", "v76", "v77", "v78", "v79", "v80", "v81", "v82", "v83", "v84", "v85", "v86", "v87", "v88", "v89", "v90", "v91", "v92", "v93", "v94", "v95", "v96", "v97", "v98", "v99", "v100", "v101", "v102", "v103", "v104", "v105", "v106", "v107", "v108", "v109", "v110", "v111", "v112", "v113", "v114", "v115", "v116", "v117", "v118", "v119", "v120", "v121", "v122", "v123", "v124", "v125", "v126", "v127", "v128", "v129", "v130", "v131", "v132", "v133", "v134", "v135", "v136", "v137", "v138", "v139", "v140", "v141", "v142", "v143", "v144", "v145", "v146", "v147", "v148", "v149", "v150", "v151", "v152", "v153", "v154", "v155", "v156", "v157", "v158", "v159", "v160", "v161", "v162", "v163", "v164", "v165", "v166", "v167", "v168", "v169", "v170", "v171", "v172", "v173", "v174", "v175", "v176", "v177", "v178", "v179", "v180", "v181", "v182", "v183", "v184", "v185", "v186", "v187", "v188", "v189", "v190", "v191", "v192", "v193", "v194", "v195", "v196", "v197", "v198", "v199", "v200", "v201", "v202", "v203", "v204", "v205", "v206", "v207", "v208", "v209", "v210", "v211", "v212", "v213", "v214", "v215", "v216", "v217", "v218", "v219", "v220", "v221", "v222", "v223", "v224", "v225", "v226", "v227", "v228", "v229", "v230", "v231", "v232", "v233", "v234", "v235", "v236", "v237", "v238", "v239", "v240", "v241", "v242", "v243", "v244", "v245", "v246", "v247", "v248", "v249", "v250", "v251", "v252", "v253", "v254", "v255", "a0", "a1", "a2", "a3", "a4", "a5", "a6", "a7", "a8", "a9", "a10", "a11", "a12", "a13", "a14", "a15", "a16", "a17", "a18", "a19", "a20", "a21", "a22", "a23", "a24", "a25", "a26", "a27", "a28", "a29", "a30", "a31", "a32", "a33", "a34", "a35", "a36", "a37", "a38", "a39", "a40", "a41", "a42", "a43", "a44", "a45", "a46", "a47", "a48", "a49", "a50", "a51", "a52", "a53", "a54", "a55", "a56", "a57", "a58", "a59", "a60", "a61", "a62", "a63", "a64", "a65", "a66", "a67", "a68", "a69", "a70", "a71", "a72", "a73", "a74", "a75", "a76", "a77", "a78", "a79", "a80", "a81", "a82", "a83", "a84", "a85", "a86", "a87", "a88", "a89", "a90", "a91", "a92", "a93", "a94", "a95", "a96", "a97", "a98", "a99", "a100", "a101", "a102", "a103", "a104", "a105", "a106", "a107", "a108", "a109", "a110", "a111", "a112", "a113", "a114", "a115", "a116", "a117", "a118", "a119", "a120", "a121", "a122", "a123", "a124", "a125", "a126", "a127", "a128", "a129", "a130", "a131", "a132", "a133", "a134", "a135", "a136", "a137", "a138", "a139", "a140", "a141", "a142", "a143", "a144", "a145", "a146", "a147", "a148", "a149", "a150", "a151", "a152", "a153", "a154", "a155", "a156", "a157", "a158", "a159", "a160", "a161", "a162", "a163", "a164", "a165", "a166", "a167", "a168", "a169", "a170", "a171", "a172", "a173", "a174", "a175", "a176", "a177", "a178", "a179", "a180", "a181", "a182", "a183", "a184", "a185", "a186", "a187", "a188", "a189", "a190", "a191", "a192", "a193", "a194", "a195", "a196", "a197", "a198", "a199", "a200", "a201", "a202", "a203", "a204", "a205", "a206", "a207", "a208", "a209", "a210", "a211", "a212", "a213", "a214", "a215", "a216", "a217", "a218", "a219", "a220", "a221", "a222", "a223", "a224", "a225", "a226", "a227", "a228", "a229", "a230", "a231", "a232", "a233", "a234", "a235", "a236", "a237", "a238", "a239", "a240", "a241", "a242", "a243", "a244", "a245", "a246", "a247", "a248", "a249", "a250", "a251", "a252", "a253", "a254", "a255", "s40", "s41", "s42", "s43", "s44", "s45", "s46", "s47", "s48", "s49", "s50", "s51", "s52", "s53", "s54", "s55", "s56", "s57", "s58", "s59", "s60", "s61", "s62", "s63", "s64", "s65", "s66", "s67", "s68", "s69", "s70", "s71", "s72", "s73", "s74", "s75", "s76", "s77", "s78", "s79", "s80", "s81", "s82", "s83", "s84", "s85", "s86", "s87", "s88", "s89", "s90", "s91", "s92", "s93", "s94", "s95", "vcc", "scc", "memory"
#define SVC_ASM \
  "s_nop 1\n" \
  "s_load_dwordx8 s[24:31], %[ka], 24\n" \
  "s_load_dwordx2 s[22:23], %[ka], 56\n" \
  "s_load_dwordx2 s[20:21], %[ka], 0\n" \
  "s_load_dwordx4 s[36:39], %[ka], 64\n" \
  "s_mov_b32 s60, %[w]\n" \
  "s_cmp_ge_u32 %[bid], 16\n" \
  "s_cselect_b32 s77, 1, 0\n" \
  "s_cselect_b32 s63, 16, 0\n" \
  "s_sub_u32 s61, %[bid], s63\n" \
  "s_mov_b32 s82, 3670016\n" \
  "s_cmp_lg_u32 s77, 0\n" \
  "s_cselect_b32 s82, 1605632, s82\n" \
  "s_cselect_b32 s83, 8, 9\n" \
  "s_lshl_b32 s83, s61, s83\n" \
  "s_mul_i32 s68, s61, 229376\n" \
  "s_cmp_lg_u32 s77, 0\n" \
  "s_cselect_b32 s63, 1, 0\n" \
  "s_lshr_b32 s68, s68, s63\n" \
  "s_cmp_eq_u32 s61, 0\n" \
  "s_cselect_b32 s78, 1.0, 0\n" \
  "s_lshl_b32 s76, s60, 10\n" \
  "s_mov_b32 s66, 32768\n" \
  "s_mov_b32 s73, 0\n" \
  "s_lshl_b32 s86, s60, 13\n" \
  "s_add_u32 s86, s86, 40960\n" \
  "s_lshl_b32 s87, s60, 12\n" \
  "s_add_u32 s87, s87, 73728\n" \
  "s_mov_b32 s70, 0x15551555\n" \
  "s_mov_b32 s71, 0x15551555\n" \
  "v_mbcnt_lo_u32_b32 v0, -1, 0\n" \
  "v_mbcnt_hi_u32_b32 v0, -1, v0\n" \
  "s_waitcnt lgkmcnt(0)\n" \
  "s_cmp_lg_u32 s77, 0\n" \
  "s_cselect_b32 s52, s22, s26\n" \
  "s_cselect_b32 s53, s23, s27\n" \
  "s_cselect_b32 s22, s28, s24\n" \
  "s_cselect_b32 s23, s29, s25\n" \
  "s_cselect_b32 s24, s30, s24\n" \
  "s_cselect_b32 s25, s31, s25\n" \
  "s_and_b32 s53, s53, 0xffff\n" \
  "s_mov_b32 s54, s82\n" \
  "s_mov_b32 s55, 0x00020000\n" \
  "s_add_u32 s44, s22, s83\n" \
  "s_addc_u32 s45, s23, 0\n" \
  "s_and_b32 s45, s45, 0xffff\n" \
  "s_sub_u32 s46, s82, s83\n" \
  "s_mov_b32 s47, 0x00020000\n" \
  "s_add_u32 s48, s24, s83\n" \
  "s_addc_u32 s49, s25, 0\n" \
  "s_and_b32 s49, s49, 0xffff\n" \
  "s_sub_u32 s50, s82, s83\n" \
  "s_mov_b32 s51, 0x00020000\n" \
  "s_mov_b32 s40, s36\n" \
  "s_and_b32 s41, s37, 0xffff\n" \
  "s_mov_b32 s42, %[xend]\n" \
  "s_mov_b32 s43, 0x00020000\n" \
  "s_mov_b32 s56, s20\n" \
  "s_and_b32 s57, s21, 0xffff\n" \
  "s_mov_b32 s58, 1792\n" \
  "s_mov_b32 s59, 0x00020000\n" \
  "s_mov_b32 s63, 1255552\n" \
  "buffer_load_dword v60, off, s[40:43], s63 sc1\n" \
  "s_cmp_eq_u32 s77, 0\n" \
  "s_cbranch_scc1 Linitw_%=\n" \
  "v_mov_b32 v56, -1\n" \
  "v_mov_b32 v57, -1\n" \
  "v_mov_b32 v58, -1\n" \
  "v_mov_b32 v59, -1\n" \
  "v_lshlrev_b32 v1, 4, v0\n" \
  "s_lshl_b32 s62, s60, 14\n" \
  "s_lshl_b32 s63, s61, 10\n" \
  "s_add_u32 s62, s62, s63\n" \
  "buffer_store_dwordx4 v[56:59], v1, s[40:43], s62 offen offset:0 sc1\n" \
  "s_lshl_b32 s72, s60, 4\n" \
  "s_add_u32 s72, s72, s61\n" \
  "s_mul_i32 s63, s72, 1792\n" \
  "s_add_u32 s62, s63, 65536\n" \
  "buffer_store_dwordx4 v[56:59], v1, s[40:43], s62 offen offset:0 sc1\n" \
  "s_mov_b32 exec_lo, 0xffffffff\n" \
  "s_mov_b32 exec_hi, 0xffff\n" \
  "buffer_store_dwordx4 v[56:59], v1, s[40:43], s62 offen offset:1024 sc1\n" \
  "s_mov_b64 exec, -1\n" \
  "s_mul_i32 s63, s72, 15232\n" \
  "s_add_u32 s62, s63, 280576\n" \
  "buffer_store_dwordx4 v[56:59], v1, s[40:43], s62 offen offset:0 sc1\n" \
  "buffer_store_dwordx4 v[56:59], v1, s[40:43], s62 offen offset:1024 sc1\n" \
  "buffer_store_dwordx4 v[56:59], v1, s[40:43], s62 offen offset:2048 sc1\n" \
  "buffer_store_dwordx4 v[56:59], v1, s[40:43], s62 offen offset:3072 sc1\n" \
  "s_add_u32 s62, s62, 4096\n" \
  "buffer_store_dwordx4 v[56:59], v1, s[40:43], s62 offen offset:0 sc1\n" \
  "buffer_store_dwordx4 v[56:59], v1, s[40:43], s62 offen offset:1024 sc1\n" \
  "buffer_store_dwordx4 v[56:59], v1, s[40:43], s62 offen offset:2048 sc1\n" \
  "buffer_store_dwordx4 v[56:59], v1, s[40:43], s62 offen offset:3072 sc1\n" \
  "s_add_u32 s62, s62, 4096\n" \
  "buffer_store_dwordx4 v[56:59], v1, s[40:43], s62 offen offset:0 sc1\n" \
  "buffer_store_dwordx4 v[56:59], v1, s[40:43], s62 offen offset:1024 sc1\n" \
  "buffer_store_dwordx4 v[56:59], v1, s[40:43], s62 offen offset:2048 sc1\n" \
  "buffer_store_dwordx4 v[56:59], v1, s[40:43], s62 offen offset:3072 sc1\n" \
  "s_add_u32 s62, s62, 4096\n" \
  "buffer_store_dwordx4 v[56:59], v1, s[40:43], s62 offen offset:0 sc1\n" \
  "buffer_store_dwordx4 v[56:59], v1, s[40:43], s62 offen offset:1024 sc1\n" \
  "s_mov_b32 exec_lo, 0xffffffff\n" \
  "s_mov_b32 exec_hi, 0xffffff\n" \
  "buffer_store_dwordx4 v[56:59], v1, s[40:43], s62 offen offset:2048 sc1\n" \
  "s_mov_b64 exec, -1\n" \
  "s_cmp_ge_u32 s61, 14\n" \
  "s_cbranch_scc1 Linit1_%=\n" \
  "s_mul_i32 s63, s60, 14\n" \
  "s_add_u32 s63, s63, s61\n" \
  "s_mul_i32 s63, s63, 1792\n" \
  "s_add_u32 s62, s63, 180224\n" \
  "buffer_store_dwordx4 v[56:59], v1, s[40:43], s62 offen offset:0 sc1\n" \
  "s_mov_b32 exec_lo, 0xffffffff\n" \
  "s_mov_b32 exec_hi, 0xffff\n" \
  "buffer_store_dwordx4 v[56:59], v1, s[40:43], s62 offen offset:1024 sc1\n" \
  "s_mov_b64 exec, -1\n" \
  "Linit1_%=:\n" \
  "s_mov_b32 exec_lo, 0xffffffff\n" \
  "s_mov_b32 exec_hi, 0xffffff\n" \
  "buffer_load_dwordx4 v[20:23], v1, s[56:59], 0 offen\n" \
  "buffer_load_dwordx4 v[24:27], v1, s[56:59], 0 offen offset:896\n" \
  "s_mov_b64 exec, -1\n" \
  "s_cmp_ge_u32 s61, 14\n" \
  "s_cbranch_scc1 Lg14_%=\n" \
  "v_lshrrev_b32 v28, 5, v0\n" \
  "v_lshlrev_b32 v30, 4, v28\n" \
  "v_lshrrev_b32 v28, 4, v0\n" \
  "v_and_b32 v28, 1, v28\n" \
  "v_lshl_add_u32 v30, v28, 2, v30\n" \
  "v_and_b32 v28, 8, v0\n" \
  "v_add_u32 v30, v30, v28\n" \
  "v_lshrrev_b32 v28, 2, v0\n" \
  "v_and_b32 v28, 1, v28\n" \
  "v_add_u32 v30, v30, v28\n" \
  "v_and_b32 v31, 3, v0\n" \
  "v_lshlrev_b32 v31, 3, v31\n" \
  "s_movk_i32 s28, 944\n" \
  "s_movk_i32 s29, 0x380\n" \
  "s_movk_i32 s30, 560\n" \
  "s_mul_i32 s63, s60, 224\n" \
  "v_mad_u32_u24 v16, v30, s28, v31\n" \
  "v_add_u32 v16, s63, v16\n" \
  "s_lshl_b32 s63, s60, 6\n" \
  "v_mad_u32_u24 v18, v30, s30, v31\n" \
  "v_add_u32 v18, s63, v18\n" \
  "v_lshrrev_b32 v28, 4, v0\n" \
  "s_lshl_b32 s63, s60, 4\n" \
  "v_add_u32 v28, s63, v28\n" \
  "v_and_b32 v29, 15, v0\n" \
  "v_lshlrev_b32 v29, 4, v29\n" \
  "v_mad_u32_u24 v17, v28, s30, v29\n" \
  "s_movk_i32 s63, 3584\n" \
  "v_mad_u32_u24 v19, v28, s63, v29\n" \
  "s_mul_i32 s82, s60, 15104\n" \
  "s_mul_i32 s83, s60, 14336\n" \
  "v_add_u32 v28, 0, v0\n" \
  "v_mul_u32_u24 v29, 0x493, v28\n" \
  "v_lshrrev_b32 v29, 16, v29\n" \
  "v_mul_u32_u24 v30, 56, v29\n" \
  "v_sub_u32 v30, v28, v30\n" \
  "v_lshlrev_b32 v30, 4, v30\n" \
  "v_mad_u32_u24 v9, v29, s28, v30\n" \
  "v_add_u32 v9, s82, v9\n" \
  "v_mad_u32_u24 v2, v29, s29, v30\n" \
  "v_add_u32 v2, s83, v2\n" \
  "v_add_u32 v28, 64, v0\n" \
  "v_mul_u32_u24 v29, 0x493, v28\n" \
  "v_lshrrev_b32 v29, 16, v29\n" \
  "v_mul_u32_u24 v30, 56, v29\n" \
  "v_sub_u32 v30, v28, v30\n" \
  "v_lshlrev_b32 v30, 4, v30\n" \
  "v_mad_u32_u24 v10, v29, s28, v30\n" \
  "v_add_u32 v10, s82, v10\n" \
  "v_mad_u32_u24 v3, v29, s29, v30\n" \
  "v_add_u32 v3, s83, v3\n" \
  "v_add_u32 v28, 128, v0\n" \
  "v_mul_u32_u24 v29, 0x493, v28\n" \
  "v_lshrrev_b32 v29, 16, v29\n" \
  "v_mul_u32_u24 v30, 56, v29\n" \
  "v_sub_u32 v30, v28, v30\n" \
  "v_lshlrev_b32 v30, 4, v30\n" \
  "v_mad_u32_u24 v11, v29, s28, v30\n" \
  "v_add_u32 v11, s82, v11\n" \
  "v_mad_u32_u24 v4, v29, s29, v30\n" \
  "v_add_u32 v4, s83, v4\n" \
  "v_add_u32 v28, 192, v0\n" \
  "v_mul_u32_u24 v29, 0x493, v28\n" \
  "v_lshrrev_b32 v29, 16, v29\n" \
  "v_mul_u32_u24 v30, 56, v29\n" \
  "v_sub_u32 v30, v28, v30\n" \
  "v_lshlrev_b32 v30, 4, v30\n" \
  "v_mad_u32_u24 v12, v29, s28, v30\n" \
  "v_add_u32 v12, s82, v12\n" \
  "v_mad_u32_u24 v5, v29, s29, v30\n" \
  "v_add_u32 v5, s83, v5\n" \
  "v_add_u32 v28, 256, v0\n" \
  "v_mul_u32_u24 v29, 0x493, v28\n" \
  "v_lshrrev_b32 v29, 16, v29\n" \
  "v_mul_u32_u24 v30, 56, v29\n" \
  "v_sub_u32 v30, v28, v30\n" \
  "v_lshlrev_b32 v30, 4, v30\n" \
  "v_mad_u32_u24 v13, v29, s28, v30\n" \
  "v_add_u32 v13, s82, v13\n" \
  "v_mad_u32_u24 v6, v29, s29, v30\n" \
  "v_add_u32 v6, s83, v6\n" \
  "v_add_u32 v28, 320, v0\n" \
  "v_mul_u32_u24 v29, 0x493, v28\n" \
  "v_lshrrev_b32 v29, 16, v29\n" \
  "v_mul_u32_u24 v30, 56, v29\n" \
  "v_sub_u32 v30, v28, v30\n" \
  "v_lshlrev_b32 v30, 4, v30\n" \
  "v_mad_u32_u24 v14, v29, s28, v30\n" \
  "v_add_u32 v14, s82, v14\n" \
  "v_mad_u32_u24 v7, v29, s29, v30\n" \
  "v_add_u32 v7, s83, v7\n" \
  "v_add_u32 v28, 384, v0\n" \
  "v_mul_u32_u24 v29, 0x493, v28\n" \
  "v_lshrrev_b32 v29, 16, v29\n" \
  "v_mul_u32_u24 v30, 56, v29\n" \
  "v_sub_u32 v30, v28, v30\n" \
  "v_lshlrev_b32 v30, 4, v30\n" \
  "v_mad_u32_u24 v15, v29, s28, v30\n" \
  "v_add_u32 v15, s82, v15\n" \
  "v_mad_u32_u24 v8, v29, s29, v30\n" \
  "v_add_u32 v8, s83, v8\n" \
  "s_mov_b32 s62, 0\n" \
  "buffer_load_dwordx4 a[0:3], v19, s[44:47], s62 offen nt\n" \
  "buffer_load_dwordx4 a[16:19], v19, s[48:51], s62 offen nt\n" \
  "s_mov_b32 s62, 14336\n" \
  "buffer_load_dwordx4 a[4:7], v19, s[44:47], s62 offen nt\n" \
  "buffer_load_dwordx4 a[20:23], v19, s[48:51], s62 offen nt\n" \
  "s_mov_b32 s62, 28672\n" \
  "buffer_load_dwordx4 a[8:11], v19, s[44:47], s62 offen nt\n" \
  "buffer_load_dwordx4 a[24:27], v19, s[48:51], s62 offen nt\n" \
  "s_mov_b32 s62, 43008\n" \
  "buffer_load_dwordx4 a[12:15], v19, s[44:47], s62 offen nt\n" \
  "buffer_load_dwordx4 a[28:31], v19, s[48:51], s62 offen nt\n" \
  "s_mov_b32 s62, 229376\n" \
  "buffer_load_dwordx4 a[32:35], v19, s[44:47], s62 offen nt\n" \
  "buffer_load_dwordx4 a[48:51], v19, s[48:51], s62 offen nt\n" \
  "s_mov_b32 s62, 243712\n" \
  "buffer_load_dwordx4 a[36:39], v19, s[44:47], s62 offen nt\n" \
  "buffer_load_dwordx4 a[52:55], v19, s[48:51], s62 offen nt\n" \
  "s_mov_b32 s62, 258048\n" \
  "buffer_load_dwordx4 a[40:43], v19, s[44:47], s62 offen nt\n" \
  "buffer_load_dwordx4 a[56:59], v19, s[48:51], s62 offen nt\n" \
  "s_mov_b32 s62, 272384\n" \
  "buffer_load_dwordx4 a[44:47], v19, s[44:47], s62 offen nt\n" \
  "buffer_load_dwordx4 a[60:63], v19, s[48:51], s62 offen nt\n" \
  "s_mov_b32 s62, 458752\n" \
  "buffer_load_dwordx4 a[64:67], v19, s[44:47], s62 offen nt\n" \
  "buffer_load_dwordx4 a[80:83], v19, s[48:51], s62 offen nt\n" \
  "s_mov_b32 s62, 473088\n" \
  "buffer_load_dwordx4 a[68:71], v19, s[44:47], s62 offen nt\n" \
  "buffer_load_dwordx4 a[84:87], v19, s[48:51], s62 offen nt\n" \
  "s_mov_b32 s62, 487424\n" \
  "buffer_load_dwordx4 a[72:75], v19, s[44:47], s62 offen nt\n" \
  "buffer_load_dwordx4 a[88:91], v19, s[48:51], s62 offen nt\n" \
  "s_mov_b32 s62, 501760\n" \
  "buffer_load_dwordx4 a[76:79], v19, s[44:47], s62 offen nt\n" \
  "buffer_load_dwordx4 a[92:95], v19, s[48:51], s62 offen nt\n" \
  "s_waitcnt vmcnt(24)\n" \
  "s_branch Lg14j_%=\n" \
  "Lg14_%=:\n" \
  "s_waitcnt vmcnt(0)\n" \
  "Lg14j_%=:\n" \
  "s_barrier\n" \
  "v_readfirstlane_b32 s80, v60\n" \
  "s_nop 3\n" \
  "s_mul_i32 s80, s80, 0x9e3779b1\n" \
  "s_add_u32 s80, s80, 0x7f4a7c15\n" \
  "s_lshl_b32 s63, s61, 2\n" \
  "s_add_u32 s63, s63, 1255424\n" \
  "v_mov_b32 v61, s80\n" \
  "s_cmp_lg_u32 s60, 0\n" \
  "s_cbranch_scc1 Linit3_%=\n" \
  "s_mov_b32 exec_lo, 0x1\n" \
  "s_mov_b32 exec_hi, 0x0\n" \
  "buffer_store_dword v61, off, s[40:43], s63 sc1\n" \
  "s_mov_b64 exec, -1\n" \
  "Linit3_%=:\n" \
  "s_cmp_ge_u32 s61, 14\n" \
  "s_cbranch_scc1 Lg15_%=\n" \
  "Lg15_%=:\n" \
  "s_nop 1\n" \
  "v_lshlrev_b32 v32, 16, v20\n" \
  "v_and_b32 v33, 0xffff0000, v20\n" \
  "v_lshlrev_b32 v34, 16, v21\n" \
  "v_and_b32 v35, 0xffff0000, v21\n" \
  "v_lshlrev_b32 v36, 16, v22\n" \
  "v_and_b32 v37, 0xffff0000, v22\n" \
  "v_lshlrev_b32 v38, 16, v23\n" \
  "v_and_b32 v39, 0xffff0000, v23\n" \
  "v_lshlrev_b32 v40, 16, v24\n" \
  "v_and_b32 v41, 0xffff0000, v24\n" \
  "v_lshlrev_b32 v42, 16, v25\n" \
  "v_and_b32 v43, 0xffff0000, v25\n" \
  "v_lshlrev_b32 v44, 16, v26\n" \
  "v_and_b32 v45, 0xffff0000, v26\n" \
  "v_lshlrev_b32 v46, 16, v27\n" \
  "v_and_b32 v47, 0xffff0000, v27\n" \
  "s_mov_b32 s74, -1\n" \
  "s_mov_b32 s75, 0x00ffffff\n" \
  "v_add_f32 v48, v32, v33\n" \
  "v_add_f32 v48, v48, v34\n" \
  "v_add_f32 v48, v48, v35\n" \
  "v_add_f32 v48, v48, v36\n" \
  "v_add_f32 v48, v48, v37\n" \
  "v_add_f32 v48, v48, v38\n" \
  "v_add_f32 v48, v48, v39\n" \
  "v_cndmask_b32_e64 v48, 0, v48, s[74:75]\n" \
  "v_add_f32 v49, v40, v41\n" \
  "v_add_f32 v49, v49, v42\n" \
  "v_add_f32 v49, v49, v43\n" \
  "v_add_f32 v49, v49, v44\n" \
  "v_add_f32 v49, v49, v45\n" \
  "v_add_f32 v49, v49, v46\n" \
  "v_add_f32 v49, v49, v47\n" \
  "v_cndmask_b32_e64 v49, 0, v49, s[74:75]\n" \
  "v_add_f32_dpp v48, v48, v48 row_ror:8 row_mask:0xf bank_mask:0xf\n" \
  "s_nop 0\n" \
  "v_add_f32_dpp v49, v49, v49 row_ror:8 row_mask:0xf bank_mask:0xf\n" \
  "v_add_f32_dpp v48, v48, v48 row_ror:4 row_mask:0xf bank_mask:0xf\n" \
  "s_nop 0\n" \
  "v_add_f32_dpp v49, v49, v49 row_ror:4 row_mask:0xf bank_mask:0xf\n" \
  "v_add_f32_dpp v48, v48, v48 row_ror:2 row_mask:0xf bank_mask:0xf\n" \
  "s_nop 0\n" \
  "v_add_f32_dpp v49, v49, v49 row_ror:2 row_mask:0xf bank_mask:0xf\n" \
  "v_add_f32_dpp v48, v48, v48 row_ror:1 row_mask:0xf bank_mask:0xf\n" \
  "s_nop 0\n" \
  "v_add_f32_dpp v49, v49, v49 row_ror:1 row_mask:0xf bank_mask:0xf\n" \
  "v_add_f32_dpp v48, v48, v48 row_bcast:15 row_mask:0xa bank_mask:0xf\n" \
  "s_nop 0\n" \
  "v_add_f32_dpp v49, v49, v49 row_bcast:15 row_mask:0xa bank_mask:0xf\n" \
  "v_add_f32_dpp v48, v48, v48 row_bcast:31 row_mask:0xc bank_mask:0xf\n" \
  "s_nop 0\n" \
  "v_add_f32_dpp v49, v49, v49 row_bcast:31 row_mask:0xc bank_mask:0xf\n" \
  "v_readlane_b32 s82, v48, 63\n" \
  "v_readlane_b32 s83, v49, 63\n" \
  "s_nop 3\n" \
  "s_mov_b32 s63, 0x3b124925\n" \
  "v_mov_b32 v50, s63\n" \
  "v_mul_f32 v20, s82, v50\n" \
  "v_mul_f32 v21, s83, v50\n" \
  "v_sub_f32 v32, v32, v20\n" \
  "v_sub_f32 v33, v33, v20\n" \
  "v_sub_f32 v34, v34, v20\n" \
  "v_sub_f32 v35, v35, v20\n" \
  "v_sub_f32 v36, v36, v20\n" \
  "v_sub_f32 v37, v37, v20\n" \
  "v_sub_f32 v38, v38, v20\n" \
  "v_sub_f32 v39, v39, v20\n" \
  "v_mul_f32 v48, v32, v32\n" \
  "v_fma_f32 v48, v33, v33, v48\n" \
  "v_fma_f32 v48, v34, v34, v48\n" \
  "v_fma_f32 v48, v35, v35, v48\n" \
  "v_fma_f32 v48, v36, v36, v48\n" \
  "v_fma_f32 v48, v37, v37, v48\n" \
  "v_fma_f32 v48, v38, v38, v48\n" \
  "v_fma_f32 v48, v39, v39, v48\n" \
  "v_cndmask_b32_e64 v48, 0, v48, s[74:75]\n" \
  "v_sub_f32 v40, v40, v21\n" \
  "v_sub_f32 v41, v41, v21\n" \
  "v_sub_f32 v42, v42, v21\n" \
  "v_sub_f32 v43, v43, v21\n" \
  "v_sub_f32 v44, v44, v21\n" \
  "v_sub_f32 v45, v45, v21\n" \
  "v_sub_f32 v46, v46, v21\n" \
  "v_sub_f32 v47, v47, v21\n" \
  "v_mul_f32 v49, v40, v40\n" \
  "v_fma_f32 v49, v41, v41, v49\n" \
  "v_fma_f32 v49, v42, v42, v49\n" \
  "v_fma_f32 v49, v43, v43, v49\n" \
  "v_fma_f32 v49, v44, v44, v49\n" \
  "v_fma_f32 v49, v45, v45, v49\n" \
  "v_fma_f32 v49, v46, v46, v49\n" \
  "v_fma_f32 v49, v47, v47, v49\n" \
  "v_cndmask_b32_e64 v49, 0, v49, s[74:75]\n" \
  "v_add_f32_dpp v48, v48, v48 row_ror:8 row_mask:0xf bank_mask:0xf\n" \
  "s_nop 0\n" \
  "v_add_f32_dpp v49, v49, v49 row_ror:8 row_mask:0xf bank_mask:0xf\n" \
  "v_add_f32_dpp v48, v48, v48 row_ror:4 row_mask:0xf bank_mask:0xf\n" \
  "s_nop 0\n" \
  "v_add_f32_dpp v49, v49, v49 row_ror:4 row_mask:0xf bank_mask:0xf\n" \
  "v_add_f32_dpp v48, v48, v48 row_ror:2 row_mask:0xf bank_mask:0xf\n" \
  "s_nop 0\n" \
  "v_add_f32_dpp v49, v49, v49 row_ror:2 row_mask:0xf bank_mask:0xf\n" \
  "v_add_f32_dpp v48, v48, v48 row_ror:1 row_mask:0xf bank_mask:0xf\n" \
  "s_nop 0\n" \
  "v_add_f32_dpp v49, v49, v49 row_ror:1 row_mask:0xf bank_mask:0xf\n" \
  "v_add_f32_dpp v48, v48, v48 row_bcast:15 row_mask:0xa bank_mask:0xf\n" \
  "s_nop 0\n" \
  "v_add_f32_dpp v49, v49, v49 row_bcast:15 row_mask:0xa bank_mask:0xf\n" \
  "v_add_f32_dpp v48, v48, v48 row_bcast:31 row_mask:0xc bank_mask:0xf\n" \
  "s_nop 0\n" \
  "v_add_f32_dpp v49, v49, v49 row_bcast:31 row_mask:0xc bank_mask:0xf\n" \
  "v_readlane_b32 s82, v48, 63\n" \
  "v_readlane_b32 s83, v49, 63\n" \
  "s_nop 3\n" \
  "v_mov_b32 v51, 0x3727c5ac\n" \
  "v_fma_f32 v22, v50, s82, v51\n" \
  "v_fma_f32 v23, v50, s83, v51\n" \
  "v_rsq_f32 v22, v22\n" \
  "v_rsq_f32 v23, v23\n" \
  "s_mov_b32 s63, 0x3db8aa3b\n" \
  "v_mul_f32 v22, s63, v22\n" \
  "v_mul_f32 v23, s63, v23\n" \
  "s_mul_i32 s67, s61, 57344\n" \
  "s_add_u32 s67, s67, 1255680\n" \
  "s_add_u32 s67, s67, s76\n" \
  "s_cmp_ge_u32 s61, 14\n" \
  "s_cbranch_scc1 Lcqr_%=\n" \
  "s_waitcnt vmcnt(16)\n" \
  "ds_write_b128 v17, a[0:3] offset:0\n" \
  "ds_write_b128 v17, a[16:19] offset:256\n" \
  "ds_write_b128 v17, a[4:7] offset:2240\n" \
  "ds_write_b128 v17, a[20:23] offset:2496\n" \
  "ds_write_b128 v17, a[8:11] offset:4480\n" \
  "ds_write_b128 v17, a[24:27] offset:4736\n" \
  "ds_write_b128 v17, a[12:15] offset:6720\n" \
  "ds_write_b128 v17, a[28:31] offset:6976\n" \
  "s_waitcnt lgkmcnt(0)\n" \
  "s_barrier\n" \
  "ds_read_b64_tr_b16 a[0:1], v18 offset:0\n" \
  "ds_read_b64_tr_b16 a[2:3], v18 offset:1120\n" \
  "ds_read_b64_tr_b16 a[4:5], v18 offset:17920\n" \
  "ds_read_b64_tr_b16 a[6:7], v18 offset:19040\n" \
  "ds_read_b64_tr_b16 a[8:9], v18 offset:32\n" \
  "ds_read_b64_tr_b16 a[10:11], v18 offset:1152\n" \
  "ds_read_b64_tr_b16 a[12:13], v18 offset:17952\n" \
  "ds_read_b64_tr_b16 a[14:15], v18 offset:19072\n" \
  "ds_read_b64_tr_b16 a[16:17], v18 offset:256\n" \
  "ds_read_b64_tr_b16 a[18:19], v18 offset:1376\n" \
  "ds_read_b64_tr_b16 a[20:21], v18 offset:18176\n" \
  "ds_read_b64_tr_b16 a[22:23], v18 offset:19296\n" \
  "ds_read_b64_tr_b16 a[24:25], v18 offset:288\n" \
  "ds_read_b64_tr_b16 a[26:27], v18 offset:1408\n" \
  "ds_read_b64_tr_b16 a[28:29], v18 offset:18208\n" \
  "ds_read_b64_tr_b16 a[30:31], v18 offset:19328\n" \
  "v_xor_b32 v9, 0x10000, v9\n" \
  "v_xor_b32 v10, 0x10000, v10\n" \
  "v_xor_b32 v11, 0x10000, v11\n" \
  "v_xor_b32 v12, 0x10000, v12\n" \
  "v_xor_b32 v13, 0x10000, v13\n" \
  "v_xor_b32 v14, 0x10000, v14\n" \
  "v_xor_b32 v15, 0x10000, v15\n" \
  "v_xor_b32 v16, 0x10000, v16\n" \
  "v_xor_b32 v17, 0x10000, v17\n" \
  "v_xor_b32 v18, 0x10000, v18\n" \
  "s_waitcnt vmcnt(8)\n" \
  "ds_write_b128 v17, a[32:35] offset:0\n" \
  "ds_write_b128 v17, a[48:51] offset:256\n" \
  "ds_write_b128 v17, a[36:39] offset:2240\n" \
  "ds_write_b128 v17, a[52:55] offset:2496\n" \
  "ds_write_b128 v17, a[40:43] offset:4480\n" \
  "ds_write_b128 v17, a[56:59] offset:4736\n" \
  "ds_write_b128 v17, a[44:47] offset:6720\n" \
  "ds_write_b128 v17, a[60:63] offset:6976\n" \
  "s_waitcnt lgkmcnt(0)\n" \
  "s_barrier\n" \
  "ds_read_b64_tr_b16 a[32:33], v18 offset:0\n" \
  "ds_read_b64_tr_b16 a[34:35], v18 offset:1120\n" \
  "ds_read_b64_tr_b16 a[36:37], v18 offset:17920\n" \
  "ds_read_b64_tr_b16 a[38:39], v18 offset:19040\n" \
  "ds_read_b64_tr_b16 a[40:41], v18 offset:32\n" \
  "ds_read_b64_tr_b16 a[42:43], v18 offset:1152\n" \
  "ds_read_b64_tr_b16 a[44:45], v18 offset:17952\n" \
  "ds_read_b64_tr_b16 a[46:47], v18 offset:19072\n" \
  "ds_read_b64_tr_b16 a[48:49], v18 offset:256\n" \
  "ds_read_b64_tr_b16 a[50:51], v18 offset:1376\n" \
  "ds_read_b64_tr_b16 a[52:53], v18 offset:18176\n" \
  "ds_read_b64_tr_b16 a[54:55], v18 offset:19296\n" \
  "ds_read_b64_tr_b16 a[56:57], v18 offset:288\n" \
  "ds_read_b64_tr_b16 a[58:59], v18 offset:1408\n" \
  "ds_read_b64_tr_b16 a[60:61], v18 offset:18208\n" \
  "ds_read_b64_tr_b16 a[62:63], v18 offset:19328\n" \
  "v_xor_b32 v9, 0x10000, v9\n" \
  "v_xor_b32 v10, 0x10000, v10\n" \
  "v_xor_b32 v11, 0x10000, v11\n" \
  "v_xor_b32 v12, 0x10000, v12\n" \
  "v_xor_b32 v13, 0x10000, v13\n" \
  "v_xor_b32 v14, 0x10000, v14\n" \
  "v_xor_b32 v15, 0x10000, v15\n" \
  "v_xor_b32 v16, 0x10000, v16\n" \
  "v_xor_b32 v17, 0x10000, v17\n" \
  "v_xor_b32 v18, 0x10000, v18\n" \
  "s_waitcnt vmcnt(0)\n" \
  "ds_write_b128 v17, a[64:67] offset:0\n" \
  "ds_write_b128 v17, a[80:83] offset:256\n" \
  "ds_write_b128 v17, a[68:71] offset:2240\n" \
  "ds_write_b128 v17, a[84:87] offset:2496\n" \
  "ds_write_b128 v17, a[72:75] offset:4480\n" \
  "ds_write_b128 v17, a[88:91] offset:4736\n" \
  "ds_write_b128 v17, a[76:79] offset:6720\n" \
  "ds_write_b128 v17, a[92:95] offset:6976\n" \
  "s_waitcnt lgkmcnt(0)\n" \
  "s_barrier\n" \
  "ds_read_b64_tr_b16 a[64:65], v18 offset:0\n" \
  "ds_read_b64_tr_b16 a[66:67], v18 offset:1120\n" \
  "ds_read_b64_tr_b16 a[68:69], v18 offset:17920\n" \
  "ds_read_b64_tr_b16 a[70:71], v18 offset:19040\n" \
  "ds_read_b64_tr_b16 a[72:73], v18 offset:32\n" \
  "ds_read_b64_tr_b16 a[74:75], v18 offset:1152\n" \
  "ds_read_b64_tr_b16 a[76:77], v18 offset:17952\n" \
  "ds_read_b64_tr_b16 a[78:79], v18 offset:19072\n" \
  "ds_read_b64_tr_b16 a[80:81], v18 offset:256\n" \
  "ds_read_b64_tr_b16 a[82:83], v18 offset:1376\n" \
  "ds_read_b64_tr_b16 a[84:85], v18 offset:18176\n" \
  "ds_read_b64_tr_b16 a[86:87], v18 offset:19296\n" \
  "ds_read_b64_tr_b16 a[88:89], v18 offset:288\n" \
  "ds_read_b64_tr_b16 a[90:91], v18 offset:1408\n" \
  "ds_read_b64_tr_b16 a[92:93], v18 offset:18208\n" \
  "ds_read_b64_tr_b16 a[94:95], v18 offset:19328\n" \
  "v_xor_b32 v9, 0x10000, v9\n" \
  "v_xor_b32 v10, 0x10000, v10\n" \
  "v_xor_b32 v11, 0x10000, v11\n" \
  "v_xor_b32 v12, 0x10000, v12\n" \
  "v_xor_b32 v13, 0x10000, v13\n" \
  "v_xor_b32 v14, 0x10000, v14\n" \
  "v_xor_b32 v15, 0x10000, v15\n" \
  "v_xor_b32 v16, 0x10000, v16\n" \
  "v_xor_b32 v17, 0x10000, v17\n" \
  "v_xor_b32 v18, 0x10000, v18\n" \
  "s_branch Lcqr_%=\n" \
  "Lcq1_%=:\n" \
  "s_nop 1\n" \
  "v_xor_b32 v48, s80, v147\n" \
  "v_xor_b32 v49, s80, v151\n" \
  "v_or_b32 v48, v48, v49\n" \
  "v_xor_b32 v49, s80, v155\n" \
  "v_or_b32 v48, v48, v49\n" \
  "v_xor_b32 v49, s80, v159\n" \
  "v_or_b32 v48, v48, v49\n" \
  "v_xor_b32 v49, s80, v163\n" \
  "v_or_b32 v48, v48, v49\n" \
  "v_xor_b32 v49, s80, v167\n" \
  "v_or_b32 v48, v48, v49\n" \
  "v_xor_b32 v49, s80, v171\n" \
  "v_or_b32 v48, v48, v49\n" \
  "v_xor_b32 v49, s80, v175\n" \
  "v_or_b32 v48, v48, v49\n" \
  "v_xor_b32 v49, s80, v179\n" \
  "v_or_b32 v48, v48, v49\n" \
  "v_xor_b32 v49, s80, v183\n" \
  "v_or_b32 v48, v48, v49\n" \
  "v_xor_b32 v49, s80, v187\n" \
  "v_or_b32 v48, v48, v49\n" \
  "v_xor_b32 v49, s80, v191\n" \
  "v_or_b32 v48, v48, v49\n" \
  "v_xor_b32 v49, s80, v195\n" \
  "v_or_b32 v48, v48, v49\n" \
  "v_xor_b32 v49, s80, v199\n" \
  "v_or_b32 v48, v48, v49\n" \
  "v_cmp_ne_u32 vcc, 0, v48\n" \
  "s_cbranch_vccz Lcq2_%=\n" \
  "s_sub_u32 s66, s66, 1\n" \
  "s_cmp_lt_i32 s66, 0\n" \
  "s_cbranch_scc1 Lcq2_%=\n" \
  "Lcqr_%=:\n" \
  "s_mov_b32 s62, s67\n" \
  "buffer_load_dwordx4 v[144:147], v1, s[40:43], s62 offen sc1\n" \
  "s_add_u32 s62, s62, 4096\n" \
  "buffer_load_dwordx4 v[148:151], v1, s[40:43], s62 offen sc1\n" \
  "s_add_u32 s62, s62, 4096\n" \
  "buffer_load_dwordx4 v[152:155], v1, s[40:43], s62 offen sc1\n" \
  "s_add_u32 s62, s62, 4096\n" \
  "buffer_load_dwordx4 v[156:159], v1, s[40:43], s62 offen sc1\n" \
  "s_add_u32 s62, s62, 4096\n" \
  "buffer_load_dwordx4 v[160:163], v1, s[40:43], s62 offen sc1\n" \
  "s_add_u32 s62, s62, 4096\n" \
  "buffer_load_dwordx4 v[164:167], v1, s[40:43], s62 offen sc1\n" \
  "s_add_u32 s62, s62, 4096\n" \
  "buffer_load_dwordx4 v[168:171], v1, s[40:43], s62 offen sc1\n" \
  "s_add_u32 s62, s62, 4096\n" \
  "buffer_load_dwordx4 v[172:175], v1, s[40:43], s62 offen sc1\n" \
  "s_add_u32 s62, s62, 4096\n" \
  "buffer_load_dwordx4 v[176:179], v1, s[40:43], s62 offen sc1\n" \
  "s_add_u32 s62, s62, 4096\n" \
  "buffer_load_dwordx4 v[180:183], v1, s[40:43], s62 offen sc1\n" \
  "s_add_u32 s62, s62, 4096\n" \
  "buffer_load_dwordx4 v[184:187], v1, s[40:43], s62 offen sc1\n" \
  "s_add_u32 s62, s62, 4096\n" \
  "buffer_load_dwordx4 v[188:191], v1, s[40:43], s62 offen sc1\n" \
  "s_add_u32 s62, s62, 4096\n" \
  "buffer_load_dwordx4 v[192:195], v1, s[40:43], s62 offen sc1\n" \
  "s_add_u32 s62, s62, 4096\n" \
  "buffer_load_dwordx4 v[196:199], v1, s[40:43], s62 offen sc1\n" \
  "s_waitcnt vmcnt(0)\n" \
  "s_branch Lcq1_%=\n" \
  "Lcq2_%=:\n" \
  "s_nop 1\n" \
  "v_pk_add_f32 v[144:145], v[144:145], v[148:149]\n" \
  "v_pk_add_f32 v[146:147], v[146:147], v[150:151]\n" \
  "v_pk_add_f32 v[152:153], v[152:153], v[156:157]\n" \
  "v_pk_add_f32 v[154:155], v[154:155], v[158:159]\n" \
  "v_pk_add_f32 v[160:161], v[160:161], v[164:165]\n" \
  "v_pk_add_f32 v[162:163], v[162:163], v[166:167]\n" \
  "v_pk_add_f32 v[168:169], v[168:169], v[172:173]\n" \
  "v_pk_add_f32 v[170:171], v[170:171], v[174:175]\n" \
  "v_pk_add_f32 v[176:177], v[176:177], v[180:181]\n" \
  "v_pk_add_f32 v[178:179], v[178:179], v[182:183]\n" \
  "v_pk_add_f32 v[184:185], v[184:185], v[188:189]\n" \
  "v_pk_add_f32 v[186:187], v[186:187], v[190:191]\n" \
  "v_pk_add_f32 v[192:193], v[192:193], v[196:197]\n" \
  "v_pk_add_f32 v[194:195], v[194:195], v[198:199]\n" \
  "v_pk_add_f32 v[144:145], v[144:145], v[152:153]\n" \
  "v_pk_add_f32 v[146:147], v[146:147], v[154:155]\n" \
  "v_pk_add_f32 v[160:161], v[160:161], v[168:169]\n" \
  "v_pk_add_f32 v[162:163], v[162:163], v[170:171]\n" \
  "v_pk_add_f32 v[176:177], v[176:177], v[184:185]\n" \
  "v_pk_add_f32 v[178:179], v[178:179], v[186:187]\n" \
  "v_pk_add_f32 v[144:145], v[144:145], v[160:161]\n" \
  "v_pk_add_f32 v[146:147], v[146:147], v[162:163]\n" \
  "v_pk_add_f32 v[176:177], v[176:177], v[192:193]\n" \
  "v_pk_add_f32 v[178:179], v[178:179], v[194:195]\n" \
  "v_pk_add_f32 v[144:145], v[144:145], v[176:177]\n" \
  "v_pk_add_f32 v[146:147], v[146:147], v[178:179]\n" \
  "v_fma_f32 v144, -v20, v146, v144\n" \
  "v_fma_f32 v145, -v21, v146, v145\n" \
  "v_mul_f32 v144, v22, v144\n" \
  "v_mul_f32 v145, v23, v145\n" \
  "s_lshl_b32 s63, s60, 7\n" \
  "v_and_b32 v38, 1, v0\n" \
  "v_lshlrev_b32 v38, 1, v38\n" \
  "v_and_b32 v39, 12, v0\n" \
  "v_add_u32 v38, v38, v39\n" \
  "v_and_b32 v39, 2, v0\n" \
  "v_lshl_add_u32 v38, v39, 4, v38\n" \
  "v_and_b32 v39, 16, v0\n" \
  "v_lshl_add_u32 v38, v39, 2, v38\n" \
  "v_and_b32 v39, 32, v0\n" \
  "v_lshrrev_b32 v39, 1, v39\n" \
  "v_add_u32 v38, v38, v39\n" \
  "v_add_u32 v38, s63, v38\n" \
  "v_cvt_pk_bf16_f32 v36, v144, v145\n" \
  "s_lshl_b32 s62, s61, 10\n" \
  "buffer_store_short v36, v38, s[40:43], s62 offen sc1\n" \
  "s_add_u32 s62, s62, 16384\n" \
  "buffer_store_short_d16_hi v36, v38, s[40:43], s62 offen sc1\n" \
  "s_cmp_ge_u32 s61, 14\n" \
  "s_cbranch_scc1 Lsend_%=\n" \
  "s_mov_b32 s62, 688128\n" \
  "buffer_load_dwordx4 a[96:99], v19, s[44:47], s62 offen nt\n" \
  "buffer_load_dwordx4 a[112:115], v19, s[48:51], s62 offen nt\n" \
  "s_mov_b32 s62, 702464\n" \
  "buffer_load_dwordx4 a[100:103], v19, s[44:47], s62 offen nt\n" \
  "buffer_load_dwordx4 a[116:119], v19, s[48:51], s62 offen nt\n" \
  "s_mov_b32 s62, 716800\n" \
  "buffer_load_dwordx4 a[104:107], v19, s[44:47], s62 offen nt\n" \
  "buffer_load_dwordx4 a[120:123], v19, s[48:51], s62 offen nt\n" \
  "s_mov_b32 s62, 731136\n" \
  "buffer_load_dwordx4 a[108:111], v19, s[44:47], s62 offen nt\n" \
  "buffer_load_dwordx4 a[124:127], v19, s[48:51], s62 offen nt\n" \
  "s_mov_b32 s62, 917504\n" \
  "buffer_load_dwordx4 a[128:131], v19, s[44:47], s62 offen nt\n" \
  "buffer_load_dwordx4 a[144:147], v19, s[48:51], s62 offen nt\n" \
  "s_mov_b32 s62, 931840\n" \
  "buffer_load_dwordx4 a[132:135], v19, s[44:47], s62 offen nt\n" \
  "buffer_load_dwordx4 a[148:151], v19, s[48:51], s62 offen nt\n" \
  "s_mov_b32 s62, 946176\n" \
  "buffer_load_dwordx4 a[136:139], v19, s[44:47], s62 offen nt\n" \
  "buffer_load_dwordx4 a[152:155], v19, s[48:51], s62 offen nt\n" \
  "s_mov_b32 s62, 960512\n" \
  "buffer_load_dwordx4 a[140:143], v19, s[44:47], s62 offen nt\n" \
  "buffer_load_dwordx4 a[156:159], v19, s[48:51], s62 offen nt\n" \
  "s_mov_b32 s62, 1146880\n" \
  "buffer_load_dwordx4 a[160:163], v19, s[44:47], s62 offen nt\n" \
  "buffer_load_dwordx4 a[176:179], v19, s[48:51], s62 offen nt\n" \
  "s_mov_b32 s62, 1161216\n" \
  "buffer_load_dwordx4 a[164:167], v19, s[44:47], s62 offen nt\n" \
  "buffer_load_dwordx4 a[180:183], v19, s[48:51], s62 offen nt\n" \
  "s_mov_b32 s62, 1175552\n" \
  "buffer_load_dwordx4 a[168:171], v19, s[44:47], s62 offen nt\n" \
  "buffer_load_dwordx4 a[184:187], v19, s[48:51], s62 offen nt\n" \
  "s_mov_b32 s62, 1189888\n" \
  "buffer_load_dwordx4 a[172:175], v19, s[44:47], s62 offen nt\n" \
  "buffer_load_dwordx4 a[188:191], v19, s[48:51], s62 offen nt\n" \
  "s_add_u32 s62, s68, 0\n" \
  "buffer_load_dwordx4 a[224:227], v2, s[52:55], s62 offen nt\n" \
  "buffer_load_dwordx4 a[228:231], v3, s[52:55], s62 offen nt\n" \
  "buffer_load_dwordx4 a[232:235], v4, s[52:55], s62 offen nt\n" \
  "buffer_load_dwordx4 a[236:239], v5, s[52:55], s62 offen nt\n" \
  "buffer_load_dwordx4 a[240:243], v6, s[52:55], s62 offen nt\n" \
  "buffer_load_dwordx4 a[244:247], v7, s[52:55], s62 offen nt\n" \
  "buffer_load_dwordx4 a[248:251], v8, s[52:55], s62 offen nt\n" \
  "s_add_u32 s62, s68, 7168\n" \
  "buffer_load_dwordx4 a[252:255], v2, s[52:55], s62 offen nt\n" \
  "buffer_load_dwordx4 v[64:67], v3, s[52:55], s62 offen nt\n" \
  "buffer_load_dwordx4 v[68:71], v4, s[52:55], s62 offen nt\n" \
  "buffer_load_dwordx4 v[72:75], v5, s[52:55], s62 offen nt\n" \
  "buffer_load_dwordx4 v[76:79], v6, s[52:55], s62 offen nt\n" \
  "buffer_load_dwordx4 v[80:83], v7, s[52:55], s62 offen nt\n" \
  "buffer_load_dwordx4 v[84:87], v8, s[52:55], s62 offen nt\n" \
  "s_mov_b32 s62, 1376256\n" \
  "buffer_load_dwordx4 a[192:195], v19, s[44:47], s62 offen nt\n" \
  "buffer_load_dwordx4 a[208:211], v19, s[48:51], s62 offen nt\n" \
  "s_mov_b32 s62, 1390592\n" \
  "buffer_load_dwordx4 a[196:199], v19, s[44:47], s62 offen nt\n" \
  "buffer_load_dwordx4 a[212:215], v19, s[48:51], s62 offen nt\n" \
  "s_mov_b32 s62, 1404928\n" \
  "buffer_load_dwordx4 a[200:203], v19, s[44:47], s62 offen nt\n" \
  "buffer_load_dwordx4 a[216:219], v19, s[48:51], s62 offen nt\n" \
  "s_mov_b32 s62, 1419264\n" \
  "buffer_load_dwordx4 a[204:207], v19, s[44:47], s62 offen nt\n" \
  "buffer_load_dwordx4 a[220:223], v19, s[48:51], s62 offen nt\n" \
  "s_add_u32 s62, s68, 57344\n" \
  "buffer_load_dwordx4 v[88:91], v2, s[52:55], s62 offen nt\n" \
  "buffer_load_dwordx4 v[92:95], v3, s[52:55], s62 offen nt\n" \
  "buffer_load_dwordx4 v[96:99], v4, s[52:55], s62 offen nt\n" \
  "buffer_load_dwordx4 v[100:103], v5, s[52:55], s62 offen nt\n" \
  "buffer_load_dwordx4 v[104:107], v6, s[52:55], s62 offen nt\n" \
  "buffer_load_dwordx4 v[108:111], v7, s[52:55], s62 offen nt\n" \
  "buffer_load_dwordx4 v[112:115], v8, s[52:55], s62 offen nt\n" \
  "s_add_u32 s62, s68, 64512\n" \
  "buffer_load_dwordx4 v[116:119], v2, s[52:55], s62 offen nt\n" \
  "buffer_load_dwordx4 v[120:123], v3, s[52:55], s62 offen nt\n" \
  "buffer_load_dwordx4 v[124:127], v4, s[52:55], s62 offen nt\n" \
  "buffer_load_dwordx4 v[128:131], v5, s[52:55], s62 offen nt\n" \
  "buffer_load_dwordx4 v[132:135], v6, s[52:55], s62 offen nt\n" \
  "buffer_load_dwordx4 v[136:139], v7, s[52:55], s62 offen nt\n" \
  "buffer_load_dwordx4 v[140:143], v8, s[52:55], s62 offen nt\n" \
  "s_waitcnt vmcnt(52)\n" \
  "ds_write_b128 v17, a[96:99] offset:0\n" \
  "ds_write_b128 v17, a[112:115] offset:256\n" \
  "ds_write_b128 v17, a[100:103] offset:2240\n" \
  "ds_write_b128 v17, a[116:119] offset:2496\n" \
  "ds_write_b128 v17, a[104:107] offset:4480\n" \
  "ds_write_b128 v17, a[120:123] offset:4736\n" \
  "ds_write_b128 v17, a[108:111] offset:6720\n" \
  "ds_write_b128 v17, a[124:127] offset:6976\n" \
  "s_waitcnt lgkmcnt(0)\n" \
  "s_barrier\n" \
  "ds_read_b64_tr_b16 a[96:97], v18 offset:0\n" \
  "ds_read_b64_tr_b16 a[98:99], v18 offset:1120\n" \
  "ds_read_b64_tr_b16 a[100:101], v18 offset:17920\n" \
  "ds_read_b64_tr_b16 a[102:103], v18 offset:19040\n" \
  "ds_read_b64_tr_b16 a[104:105], v18 offset:32\n" \
  "ds_read_b64_tr_b16 a[106:107], v18 offset:1152\n" \
  "ds_read_b64_tr_b16 a[108:109], v18 offset:17952\n" \
  "ds_read_b64_tr_b16 a[110:111], v18 offset:19072\n" \
  "ds_read_b64_tr_b16 a[112:113], v18 offset:256\n" \
  "ds_read_b64_tr_b16 a[114:115], v18 offset:1376\n" \
  "ds_read_b64_tr_b16 a[116:117], v18 offset:18176\n" \
  "ds_read_b64_tr_b16 a[118:119], v18 offset:19296\n" \
  "ds_read_b64_tr_b16 a[120:121], v18 offset:288\n" \
  "ds_read_b64_tr_b16 a[122:123], v18 offset:1408\n" \
  "ds_read_b64_tr_b16 a[124:125], v18 offset:18208\n" \
  "ds_read_b64_tr_b16 a[126:127], v18 offset:19328\n" \
  "v_xor_b32 v9, 0x10000, v9\n" \
  "v_xor_b32 v10, 0x10000, v10\n" \
  "v_xor_b32 v11, 0x10000, v11\n" \
  "v_xor_b32 v12, 0x10000, v12\n" \
  "v_xor_b32 v13, 0x10000, v13\n" \
  "v_xor_b32 v14, 0x10000, v14\n" \
  "v_xor_b32 v15, 0x10000, v15\n" \
  "v_xor_b32 v16, 0x10000, v16\n" \
  "v_xor_b32 v17, 0x10000, v17\n" \
  "v_xor_b32 v18, 0x10000, v18\n" \
  "s_waitcnt vmcnt(44)\n" \
  "ds_write_b128 v17, a[128:131] offset:0\n" \
  "ds_write_b128 v17, a[144:147] offset:256\n" \
  "ds_write_b128 v17, a[132:135] offset:2240\n" \
  "ds_write_b128 v17, a[148:151] offset:2496\n" \
  "ds_write_b128 v17, a[136:139] offset:4480\n" \
  "ds_write_b128 v17, a[152:155] offset:4736\n" \
  "ds_write_b128 v17, a[140:143] offset:6720\n" \
  "ds_write_b128 v17, a[156:159] offset:6976\n" \
  "s_waitcnt lgkmcnt(0)\n" \
  "s_barrier\n" \
  "ds_read_b64_tr_b16 a[128:129], v18 offset:0\n" \
  "ds_read_b64_tr_b16 a[130:131], v18 offset:1120\n" \
  "ds_read_b64_tr_b16 a[132:133], v18 offset:17920\n" \
  "ds_read_b64_tr_b16 a[134:135], v18 offset:19040\n" \
  "ds_read_b64_tr_b16 a[136:137], v18 offset:32\n" \
  "ds_read_b64_tr_b16 a[138:139], v18 offset:1152\n" \
  "ds_read_b64_tr_b16 a[140:141], v18 offset:17952\n" \
  "ds_read_b64_tr_b16 a[142:143], v18 offset:19072\n" \
  "ds_read_b64_tr_b16 a[144:145], v18 offset:256\n" \
  "ds_read_b64_tr_b16 a[146:147], v18 offset:1376\n" \
  "ds_read_b64_tr_b16 a[148:149], v18 offset:18176\n" \
  "ds_read_b64_tr_b16 a[150:151], v18 offset:19296\n" \
  "ds_read_b64_tr_b16 a[152:153], v18 offset:288\n" \
  "ds_read_b64_tr_b16 a[154:155], v18 offset:1408\n" \
  "ds_read_b64_tr_b16 a[156:157], v18 offset:18208\n" \
  "ds_read_b64_tr_b16 a[158:159], v18 offset:19328\n" \
  "v_xor_b32 v9, 0x10000, v9\n" \
  "v_xor_b32 v10, 0x10000, v10\n" \
  "v_xor_b32 v11, 0x10000, v11\n" \
  "v_xor_b32 v12, 0x10000, v12\n" \
  "v_xor_b32 v13, 0x10000, v13\n" \
  "v_xor_b32 v14, 0x10000, v14\n" \
  "v_xor_b32 v15, 0x10000, v15\n" \
  "v_xor_b32 v16, 0x10000, v16\n" \
  "v_xor_b32 v17, 0x10000, v17\n" \
  "v_xor_b32 v18, 0x10000, v18\n" \
  "s_waitcnt vmcnt(36)\n" \
  "ds_write_b128 v17, a[160:163] offset:0\n" \
  "ds_write_b128 v17, a[176:179] offset:256\n" \
  "ds_write_b128 v17, a[164:167] offset:2240\n" \
  "ds_write_b128 v17, a[180:183] offset:2496\n" \
  "ds_write_b128 v17, a[168:171] offset:4480\n" \
  "ds_write_b128 v17, a[184:187] offset:4736\n" \
  "ds_write_b128 v17, a[172:175] offset:6720\n" \
  "ds_write_b128 v17, a[188:191] offset:6976\n" \
  "s_waitcnt lgkmcnt(0)\n" \
  "s_barrier\n" \
  "ds_read_b64_tr_b16 a[160:161], v18 offset:0\n" \
  "ds_read_b64_tr_b16 a[162:163], v18 offset:1120\n" \
  "ds_read_b64_tr_b16 a[164:165], v18 offset:17920\n" \
  "ds_read_b64_tr_b16 a[166:167], v18 offset:19040\n" \
  "ds_read_b64_tr_b16 a[168:169], v18 offset:32\n" \
  "ds_read_b64_tr_b16 a[170:171], v18 offset:1152\n" \
  "ds_read_b64_tr_b16 a[172:173], v18 offset:17952\n" \
  "ds_read_b64_tr_b16 a[174:175], v18 offset:19072\n" \
  "ds_read_b64_tr_b16 a[176:177], v18 offset:256\n" \
  "ds_read_b64_tr_b16 a[178:179], v18 offset:1376\n" \
  "ds_read_b64_tr_b16 a[180:181], v18 offset:18176\n" \
  "ds_read_b64_tr_b16 a[182:183], v18 offset:19296\n" \
  "ds_read_b64_tr_b16 a[184:185], v18 offset:288\n" \
  "ds_read_b64_tr_b16 a[186:187], v18 offset:1408\n" \
  "ds_read_b64_tr_b16 a[188:189], v18 offset:18208\n" \
  "ds_read_b64_tr_b16 a[190:191], v18 offset:19328\n" \
  "v_xor_b32 v9, 0x10000, v9\n" \
  "v_xor_b32 v10, 0x10000, v10\n" \
  "v_xor_b32 v11, 0x10000, v11\n" \
  "v_xor_b32 v12, 0x10000, v12\n" \
  "v_xor_b32 v13, 0x10000, v13\n" \
  "v_xor_b32 v14, 0x10000, v14\n" \
  "v_xor_b32 v15, 0x10000, v15\n" \
  "v_xor_b32 v16, 0x10000, v16\n" \
  "v_xor_b32 v17, 0x10000, v17\n" \
  "v_xor_b32 v18, 0x10000, v18\n" \
  "s_waitcnt vmcnt(14)\n" \
  "ds_write_b128 v17, a[192:195] offset:0\n" \
  "ds_write_b128 v17, a[208:211] offset:256\n" \
  "ds_write_b128 v17, a[196:199] offset:2240\n" \
  "ds_write_b128 v17, a[212:215] offset:2496\n" \
  "ds_write_b128 v17, a[200:203] offset:4480\n" \
  "ds_write_b128 v17, a[216:219] offset:4736\n" \
  "ds_write_b128 v17, a[204:207] offset:6720\n" \
  "ds_write_b128 v17, a[220:223] offset:6976\n" \
  "s_waitcnt lgkmcnt(0)\n" \
  "s_barrier\n" \
  "ds_read_b64_tr_b16 a[192:193], v18 offset:0\n" \
  "ds_read_b64_tr_b16 a[194:195], v18 offset:1120\n" \
  "ds_read_b64_tr_b16 a[196:197], v18 offset:17920\n" \
  "ds_read_b64_tr_b16 a[198:199], v18 offset:19040\n" \
  "ds_read_b64_tr_b16 a[200:201], v18 offset:32\n" \
  "ds_read_b64_tr_b16 a[202:203], v18 offset:1152\n" \
  "ds_read_b64_tr_b16 a[204:205], v18 offset:17952\n" \
  "ds_read_b64_tr_b16 a[206:207], v18 offset:19072\n" \
  "ds_read_b64_tr_b16 a[208:209], v18 offset:256\n" \
  "ds_read_b64_tr_b16 a[210:211], v18 offset:1376\n" \
  "ds_read_b64_tr_b16 a[212:213], v18 offset:18176\n" \
  "ds_read_b64_tr_b16 a[214:215], v18 offset:19296\n" \
  "ds_read_b64_tr_b16 a[216:217], v18 offset:288\n" \
  "ds_read_b64_tr_b16 a[218:219], v18 offset:1408\n" \
  "ds_read_b64_tr_b16 a[220:221], v18 offset:18208\n" \
  "ds_read_b64_tr_b16 a[222:223], v18 offset:19328\n" \
  "v_xor_b32 v9, 0x10000, v9\n" \
  "v_xor_b32 v10, 0x10000, v10\n" \
  "v_xor_b32 v11, 0x10000, v11\n" \
  "v_xor_b32 v12, 0x10000, v12\n" \
  "v_xor_b32 v13, 0x10000, v13\n" \
  "v_xor_b32 v14, 0x10000, v14\n" \
  "v_xor_b32 v15, 0x10000, v15\n" \
  "v_xor_b32 v16, 0x10000, v16\n" \
  "v_xor_b32 v17, 0x10000, v17\n" \
  "v_xor_b32 v18, 0x10000, v18\n" \
  "ds_write_b128 v9, a[224:227] offset:0\n" \
  "ds_write_b128 v10, a[228:231] offset:0\n" \
  "ds_write_b128 v11, a[232:235] offset:0\n" \
  "ds_write_b128 v12, a[236:239] offset:0\n" \
  "ds_write_b128 v13, a[240:243] offset:0\n" \
  "ds_write_b128 v14, a[244:247] offset:0\n" \
  "ds_write_b128 v15, a[248:251] offset:0\n" \
  "ds_write_b128 v9, a[252:255] offset:7552\n" \
  "ds_write_b128 v10, v[64:67] offset:7552\n" \
  "ds_write_b128 v11, v[68:71] offset:7552\n" \
  "ds_write_b128 v12, v[72:75] offset:7552\n" \
  "ds_write_b128 v13, v[76:79] offset:7552\n" \
  "ds_write_b128 v14, v[80:83] offset:7552\n" \
  "ds_write_b128 v15, v[84:87] offset:7552\n" \
  "s_waitcnt lgkmcnt(0)\n" \
  "s_barrier\n" \
  "ds_read_b64_tr_b16 a[224:225], v16 offset:0\n" \
  "ds_read_b64_tr_b16 a[226:227], v16 offset:1888\n" \
  "ds_read_b64_tr_b16 a[228:229], v16 offset:30208\n" \
  "ds_read_b64_tr_b16 a[230:231], v16 offset:32096\n" \
  "ds_read_b64_tr_b16 a[232:233], v16 offset:32\n" \
  "ds_read_b64_tr_b16 a[234:235], v16 offset:1920\n" \
  "ds_read_b64_tr_b16 a[236:237], v16 offset:30240\n" \
  "ds_read_b64_tr_b16 a[238:239], v16 offset:32128\n" \
  "ds_read_b64_tr_b16 a[240:241], v16 offset:64\n" \
  "ds_read_b64_tr_b16 a[242:243], v16 offset:1952\n" \
  "ds_read_b64_tr_b16 a[244:245], v16 offset:30272\n" \
  "ds_read_b64_tr_b16 a[246:247], v16 offset:32160\n" \
  "ds_read_b64_tr_b16 a[248:249], v16 offset:96\n" \
  "ds_read_b64_tr_b16 a[250:251], v16 offset:1984\n" \
  "ds_read_b64_tr_b16 a[252:253], v16 offset:30304\n" \
  "ds_read_b64_tr_b16 a[254:255], v16 offset:32192\n" \
  "ds_read_b64_tr_b16 v[64:65], v16 offset:128\n" \
  "ds_read_b64_tr_b16 v[66:67], v16 offset:2016\n" \
  "ds_read_b64_tr_b16 v[68:69], v16 offset:30336\n" \
  "ds_read_b64_tr_b16 v[70:71], v16 offset:32224\n" \
  "ds_read_b64_tr_b16 v[72:73], v16 offset:160\n" \
  "ds_read_b64_tr_b16 v[74:75], v16 offset:2048\n" \
  "ds_read_b64_tr_b16 v[76:77], v16 offset:30368\n" \
  "ds_read_b64_tr_b16 v[78:79], v16 offset:32256\n" \
  "ds_read_b64_tr_b16 v[80:81], v16 offset:192\n" \
  "ds_read_b64_tr_b16 v[82:83], v16 offset:2080\n" \
  "ds_read_b64_tr_b16 v[84:85], v16 offset:30400\n" \
  "ds_read_b64_tr_b16 v[86:87], v16 offset:32288\n" \
  "v_xor_b32 v9, 0x10000, v9\n" \
  "v_xor_b32 v10, 0x10000, v10\n" \
  "v_xor_b32 v11, 0x10000, v11\n" \
  "v_xor_b32 v12, 0x10000, v12\n" \
  "v_xor_b32 v13, 0x10000, v13\n" \
  "v_xor_b32 v14, 0x10000, v14\n" \
  "v_xor_b32 v15, 0x10000, v15\n" \
  "v_xor_b32 v16, 0x10000, v16\n" \
  "v_xor_b32 v17, 0x10000, v17\n" \
  "v_xor_b32 v18, 0x10000, v18\n" \
  "s_waitcnt vmcnt(0)\n" \
  "ds_write_b128 v9, v[88:91] offset:0\n" \
  "ds_write_b128 v10, v[92:95] offset:0\n" \
  "ds_write_b128 v11, v[96:99] offset:0\n" \
  "ds_write_b128 v12, v[100:103] offset:0\n" \
  "ds_write_b128 v13, v[104:107] offset:0\n" \
  "ds_write_b128 v14, v[108:111] offset:0\n" \
  "ds_write_b128 v15, v[112:115] offset:0\n" \
  "ds_write_b128 v9, v[116:119] offset:7552\n" \
  "ds_write_b128 v10, v[120:123] offset:7552\n" \
  "ds_write_b128 v11, v[124:127] offset:7552\n" \
  "ds_write_b128 v12, v[128:131] offset:7552\n" \
  "ds_write_b128 v13, v[132:135] offset:7552\n" \
  "ds_write_b128 v14, v[136:139] offset:7552\n" \
  "ds_write_b128 v15, v[140:143] offset:7552\n" \
  "s_waitcnt lgkmcnt(0)\n" \
  "s_barrier\n" \
  "ds_read_b64_tr_b16 v[88:89], v16 offset:0\n" \
  "ds_read_b64_tr_b16 v[90:91], v16 offset:1888\n" \
  "ds_read_b64_tr_b16 v[92:93], v16 offset:30208\n" \
  "ds_read_b64_tr_b16 v[94:95], v16 offset:32096\n" \
  "ds_read_b64_tr_b16 v[96:97], v16 offset:32\n" \
  "ds_read_b64_tr_b16 v[98:99], v16 offset:1920\n" \
  "ds_read_b64_tr_b16 v[100:101], v16 offset:30240\n" \
  "ds_read_b64_tr_b16 v[102:103], v16 offset:32128\n" \
  "ds_read_b64_tr_b16 v[104:105], v16 offset:64\n" \
  "ds_read_b64_tr_b16 v[106:107], v16 offset:1952\n" \
  "ds_read_b64_tr_b16 v[108:109], v16 offset:30272\n" \
  "ds_read_b64_tr_b16 v[110:111], v16 offset:32160\n" \
  "ds_read_b64_tr_b16 v[112:113], v16 offset:96\n" \
  "ds_read_b64_tr_b16 v[114:115], v16 offset:1984\n" \
  "ds_read_b64_tr_b16 v[116:117], v16 offset:30304\n" \
  "ds_read_b64_tr_b16 v[118:119], v16 offset:32192\n" \
  "ds_read_b64_tr_b16 v[120:121], v16 offset:128\n" \
  "ds_read_b64_tr_b16 v[122:123], v16 offset:2016\n" \
  "ds_read_b64_tr_b16 v[124:125], v16 offset:30336\n" \
  "ds_read_b64_tr_b16 v[126:127], v16 offset:32224\n" \
  "ds_read_b64_tr_b16 v[128:129], v16 offset:160\n" \
  "ds_read_b64_tr_b16 v[130:131], v16 offset:2048\n" \
  "ds_read_b64_tr_b16 v[132:133], v16 offset:30368\n" \
  "ds_read_b64_tr_b16 v[134:135], v16 offset:32256\n" \
  "ds_read_b64_tr_b16 v[136:137], v16 offset:192\n" \
  "ds_read_b64_tr_b16 v[138:139], v16 offset:2080\n" \
  "ds_read_b64_tr_b16 v[140:141], v16 offset:30400\n" \
  "ds_read_b64_tr_b16 v[142:143], v16 offset:32288\n" \
  "v_xor_b32 v9, 0x10000, v9\n" \
  "v_xor_b32 v10, 0x10000, v10\n" \
  "v_xor_b32 v11, 0x10000, v11\n" \
  "v_xor_b32 v12, 0x10000, v12\n" \
  "v_xor_b32 v13, 0x10000, v13\n" \
  "v_xor_b32 v14, 0x10000, v14\n" \
  "v_xor_b32 v15, 0x10000, v15\n" \
  "v_xor_b32 v16, 0x10000, v16\n" \
  "v_xor_b32 v17, 0x10000, v17\n" \
  "v_xor_b32 v18, 0x10000, v18\n" \
  "s_mov_b32 s56, s38\n" \
  "s_and_b32 s57, s39, 0xffff\n" \
  "s_mov_b32 s58, 1792\n" \
  "s_mov_b32 s59, 0x00020000\n" \
  "s_branch Lldj_%=\n" \
  "Linitw_%=:\n" \
  "s_nop 1\n" \
  "v_and_b32 v32, 15, v0\n" \
  "v_lshrrev_b32 v33, 4, v0\n" \
  "v_lshrrev_b32 v34, 1, v32\n" \
  "v_min_u32 v34, 6, v34\n" \
  "v_and_b32 v35, 1, v32\n" \
  "s_mul_i32 s63, s60, 28\n" \
  "v_mul_u32_u24 v36, 7, v33\n" \
  "v_add_u32 v36, s63, v36\n" \
  "v_add_u32 v37, v36, v34\n" \
  "v_lshlrev_b32 v38, 3, v37\n" \
  "buffer_load_dwordx2 v[46:47], v38, s[56:59], 0 offen offset:0\n" \
  "buffer_load_dwordx2 v[48:49], v38, s[56:59], 0 offen offset:896\n" \
  "v_lshrrev_b32 v28, 5, v0\n" \
  "v_lshlrev_b32 v30, 4, v28\n" \
  "v_lshrrev_b32 v28, 4, v0\n" \
  "v_and_b32 v28, 1, v28\n" \
  "v_lshl_add_u32 v30, v28, 2, v30\n" \
  "v_and_b32 v28, 8, v0\n" \
  "v_add_u32 v30, v30, v28\n" \
  "v_lshrrev_b32 v28, 2, v0\n" \
  "v_and_b32 v28, 1, v28\n" \
  "v_add_u32 v30, v30, v28\n" \
  "v_and_b32 v31, 3, v0\n" \
  "v_lshlrev_b32 v31, 3, v31\n" \
  "s_movk_i32 s28, 944\n" \
  "s_movk_i32 s29, 0x380\n" \
  "s_movk_i32 s30, 560\n" \
  "s_mul_i32 s63, s60, 224\n" \
  "v_mad_u32_u24 v16, v30, s28, v31\n" \
  "v_add_u32 v16, s63, v16\n" \
  "s_lshl_b32 s63, s60, 7\n" \
  "v_mad_u32_u24 v18, v30, s30, v31\n" \
  "v_add_u32 v18, s63, v18\n" \
  "v_lshrrev_b32 v28, 5, v0\n" \
  "s_lshl_b32 s63, s60, 4\n" \
  "v_add_u32 v28, s63, v28\n" \
  "v_and_b32 v29, 31, v0\n" \
  "v_lshlrev_b32 v29, 4, v29\n" \
  "v_mad_u32_u24 v17, v28, s30, v29\n" \
  "s_movk_i32 s63, 8192\n" \
  "v_mad_u32_u24 v19, v28, s63, v29\n" \
  "s_mul_i32 s82, s60, 15104\n" \
  "s_mul_i32 s83, s60, 14336\n" \
  "v_add_u32 v28, 0, v0\n" \
  "v_mul_u32_u24 v29, 0x493, v28\n" \
  "v_lshrrev_b32 v29, 16, v29\n" \
  "v_mul_u32_u24 v30, 56, v29\n" \
  "v_sub_u32 v30, v28, v30\n" \
  "v_lshlrev_b32 v30, 4, v30\n" \
  "v_mad_u32_u24 v9, v29, s28, v30\n" \
  "v_add_u32 v9, s82, v9\n" \
  "v_mad_u32_u24 v2, v29, s29, v30\n" \
  "v_add_u32 v2, s83, v2\n" \
  "v_add_u32 v28, 64, v0\n" \
  "v_mul_u32_u24 v29, 0x493, v28\n" \
  "v_lshrrev_b32 v29, 16, v29\n" \
  "v_mul_u32_u24 v30, 56, v29\n" \
  "v_sub_u32 v30, v28, v30\n" \
  "v_lshlrev_b32 v30, 4, v30\n" \
  "v_mad_u32_u24 v10, v29, s28, v30\n" \
  "v_add_u32 v10, s82, v10\n" \
  "v_mad_u32_u24 v3, v29, s29, v30\n" \
  "v_add_u32 v3, s83, v3\n" \
  "v_add_u32 v28, 128, v0\n" \
  "v_mul_u32_u24 v29, 0x493, v28\n" \
  "v_lshrrev_b32 v29, 16, v29\n" \
  "v_mul_u32_u24 v30, 56, v29\n" \
  "v_sub_u32 v30, v28, v30\n" \
  "v_lshlrev_b32 v30, 4, v30\n" \
  "v_mad_u32_u24 v11, v29, s28, v30\n" \
  "v_add_u32 v11, s82, v11\n" \
  "v_mad_u32_u24 v4, v29, s29, v30\n" \
  "v_add_u32 v4, s83, v4\n" \
  "v_add_u32 v28, 192, v0\n" \
  "v_mul_u32_u24 v29, 0x493, v28\n" \
  "v_lshrrev_b32 v29, 16, v29\n" \
  "v_mul_u32_u24 v30, 56, v29\n" \
  "v_sub_u32 v30, v28, v30\n" \
  "v_lshlrev_b32 v30, 4, v30\n" \
  "v_mad_u32_u24 v12, v29, s28, v30\n" \
  "v_add_u32 v12, s82, v12\n" \
  "v_mad_u32_u24 v5, v29, s29, v30\n" \
  "v_add_u32 v5, s83, v5\n" \
  "v_add_u32 v28, 256, v0\n" \
  "v_mul_u32_u24 v29, 0x493, v28\n" \
  "v_lshrrev_b32 v29, 16, v29\n" \
  "v_mul_u32_u24 v30, 56, v29\n" \
  "v_sub_u32 v30, v28, v30\n" \
  "v_lshlrev_b32 v30, 4, v30\n" \
  "v_mad_u32_u24 v13, v29, s28, v30\n" \
  "v_add_u32 v13, s82, v13\n" \
  "v_mad_u32_u24 v6, v29, s29, v30\n" \
  "v_add_u32 v6, s83, v6\n" \
  "v_add_u32 v28, 320, v0\n" \
  "v_mul_u32_u24 v29, 0x493, v28\n" \
  "v_lshrrev_b32 v29, 16, v29\n" \
  "v_mul_u32_u24 v30, 56, v29\n" \
  "v_sub_u32 v30, v28, v30\n" \
  "v_lshlrev_b32 v30, 4, v30\n" \
  "v_mad_u32_u24 v14, v29, s28, v30\n" \
  "v_add_u32 v14, s82, v14\n" \
  "v_mad_u32_u24 v7, v29, s29, v30\n" \
  "v_add_u32 v7, s83, v7\n" \
  "v_add_u32 v28, 384, v0\n" \
  "v_mul_u32_u24 v29, 0x493, v28\n" \
  "v_lshrrev_b32 v29, 16, v29\n" \
  "v_mul_u32_u24 v30, 56, v29\n" \
  "v_sub_u32 v30, v28, v30\n" \
  "v_lshlrev_b32 v30, 4, v30\n" \
  "v_mad_u32_u24 v15, v29, s28, v30\n" \
  "v_add_u32 v15, s82, v15\n" \
  "v_mad_u32_u24 v8, v29, s29, v30\n" \
  "v_add_u32 v8, s83, v8\n" \
  "s_add_u32 s62, s68, 0\n" \
  "buffer_load_dwordx4 a[224:227], v2, s[52:55], s62 offen nt\n" \
  "buffer_load_dwordx4 a[228:231], v3, s[52:55], s62 offen nt\n" \
  "buffer_load_dwordx4 a[232:235], v4, s[52:55], s62 offen nt\n" \
  "buffer_load_dwordx4 a[236:239], v5, s[52:55], s62 offen nt\n" \
  "buffer_load_dwordx4 a[240:243], v6, s[52:55], s62 offen nt\n" \
  "buffer_load_dwordx4 a[244:247], v7, s[52:55], s62 offen nt\n" \
  "buffer_load_dwordx4 a[248:251], v8, s[52:55], s62 offen nt\n" \
  "s_add_u32 s62, s68, 7168\n" \
  "buffer_load_dwordx4 a[252:255], v2, s[52:55], s62 offen nt\n" \
  "buffer_load_dwordx4 v[64:67], v3, s[52:55], s62 offen nt\n" \
  "buffer_load_dwordx4 v[68:71], v4, s[52:55], s62 offen nt\n" \
  "buffer_load_dwordx4 v[72:75], v5, s[52:55], s62 offen nt\n" \
  "buffer_load_dwordx4 v[76:79], v6, s[52:55], s62 offen nt\n" \
  "buffer_load_dwordx4 v[80:83], v7, s[52:55], s62 offen nt\n" \
  "buffer_load_dwordx4 v[84:87], v8, s[52:55], s62 offen nt\n" \
  "s_add_u32 s62, s68, 57344\n" \
  "buffer_load_dwordx4 v[88:91], v2, s[52:55], s62 offen nt\n" \
  "buffer_load_dwordx4 v[92:95], v3, s[52:55], s62 offen nt\n" \
  "buffer_load_dwordx4 v[96:99], v4, s[52:55], s62 offen nt\n" \
  "buffer_load_dwordx4 v[100:103], v5, s[52:55], s62 offen nt\n" \
  "buffer_load_dwordx4 v[104:107], v6, s[52:55], s62 offen nt\n" \
  "buffer_load_dwordx4 v[108:111], v7, s[52:55], s62 offen nt\n" \
  "buffer_load_dwordx4 v[112:115], v8, s[52:55], s62 offen nt\n" \
  "s_add_u32 s62, s68, 64512\n" \
  "buffer_load_dwordx4 v[116:119], v2, s[52:55], s62 offen nt\n" \
  "buffer_load_dwordx4 v[120:123], v3, s[52:55], s62 offen nt\n" \
  "buffer_load_dwordx4 v[124:127], v4, s[52:55], s62 offen nt\n" \
  "buffer_load_dwordx4 v[128:131], v5, s[52:55], s62 offen nt\n" \
  "buffer_load_dwordx4 v[132:135], v6, s[52:55], s62 offen nt\n" \
  "buffer_load_dwordx4 v[136:139], v7, s[52:55], s62 offen nt\n" \
  "buffer_load_dwordx4 v[140:143], v8, s[52:55], s62 offen nt\n" \
  "s_add_u32 s62, s68, 114688\n" \
  "buffer_load_dwordx4 v[144:147], v2, s[52:55], s62 offen nt\n" \
  "buffer_load_dwordx4 v[148:151], v3, s[52:55], s62 offen nt\n" \
  "buffer_load_dwordx4 v[152:155], v4, s[52:55], s62 offen nt\n" \
  "buffer_load_dwordx4 v[156:159], v5, s[52:55], s62 offen nt\n" \
  "buffer_load_dwordx4 v[160:163], v6, s[52:55], s62 offen nt\n" \
  "buffer_load_dwordx4 v[164:167], v7, s[52:55], s62 offen nt\n" \
  "buffer_load_dwordx4 v[168:171], v8, s[52:55], s62 offen nt\n" \
  "s_add_u32 s62, s68, 121856\n" \
  "buffer_load_dwordx4 v[172:175], v2, s[52:55], s62 offen nt\n" \
  "buffer_load_dwordx4 v[176:179], v3, s[52:55], s62 offen nt\n" \
  "buffer_load_dwordx4 v[180:183], v4, s[52:55], s62 offen nt\n" \
  "buffer_load_dwordx4 v[184:187], v5, s[52:55], s62 offen nt\n" \
  "buffer_load_dwordx4 v[188:191], v6, s[52:55], s62 offen nt\n" \
  "buffer_load_dwordx4 v[192:195], v7, s[52:55], s62 offen nt\n" \
  "buffer_load_dwordx4 v[196:199], v8, s[52:55], s62 offen nt\n" \
  "s_add_u32 s62, s68, 172032\n" \
  "buffer_load_dwordx4 v[200:203], v2, s[52:55], s62 offen nt\n" \
  "buffer_load_dwordx4 v[204:207], v3, s[52:55], s62 offen nt\n" \
  "buffer_load_dwordx4 v[208:211], v4, s[52:55], s62 offen nt\n" \
  "buffer_load_dwordx4 v[212:215], v5, s[52:55], s62 offen nt\n" \
  "buffer_load_dwordx4 v[216:219], v6, s[52:55], s62 offen nt\n" \
  "buffer_load_dwordx4 v[220:223], v7, s[52:55], s62 offen nt\n" \
  "buffer_load_dwordx4 v[224:227], v8, s[52:55], s62 offen nt\n" \
  "s_add_u32 s62, s68, 179200\n" \
  "buffer_load_dwordx4 v[228:231], v2, s[52:55], s62 offen nt\n" \
  "buffer_load_dwordx4 v[232:235], v3, s[52:55], s62 offen nt\n" \
  "buffer_load_dwordx4 v[236:239], v4, s[52:55], s62 offen nt\n" \
  "buffer_load_dwordx4 v[240:243], v5, s[52:55], s62 offen nt\n" \
  "buffer_load_dwordx4 v[244:247], v6, s[52:55], s62 offen nt\n" \
  "buffer_load_dwordx4 v[248:251], v7, s[52:55], s62 offen nt\n" \
  "buffer_load_dwordx4 v[252:255], v8, s[52:55], s62 offen nt\n" \
  "s_waitcnt vmcnt(42)\n" \
  "ds_write_b128 v9, a[224:227] offset:0\n" \
  "ds_write_b128 v10, a[228:231] offset:0\n" \
  "ds_write_b128 v11, a[232:235] offset:0\n" \
  "ds_write_b128 v12, a[236:239] offset:0\n" \
  "ds_write_b128 v13, a[240:243] offset:0\n" \
  "ds_write_b128 v14, a[244:247] offset:0\n" \
  "ds_write_b128 v15, a[248:251] offset:0\n" \
  "ds_write_b128 v9, a[252:255] offset:7552\n" \
  "ds_write_b128 v10, v[64:67] offset:7552\n" \
  "ds_write_b128 v11, v[68:71] offset:7552\n" \
  "ds_write_b128 v12, v[72:75] offset:7552\n" \
  "ds_write_b128 v13, v[76:79] offset:7552\n" \
  "ds_write_b128 v14, v[80:83] offset:7552\n" \
  "ds_write_b128 v15, v[84:87] offset:7552\n" \
  "s_waitcnt lgkmcnt(0)\n" \
  "s_barrier\n" \
  "ds_read_b64_tr_b16 a[224:225], v16 offset:0\n" \
  "ds_read_b64_tr_b16 a[226:227], v16 offset:1888\n" \
  "ds_read_b64_tr_b16 a[228:229], v16 offset:30208\n" \
  "ds_read_b64_tr_b16 a[230:231], v16 offset:32096\n" \
  "ds_read_b64_tr_b16 a[232:233], v16 offset:32\n" \
  "ds_read_b64_tr_b16 a[234:235], v16 offset:1920\n" \
  "ds_read_b64_tr_b16 a[236:237], v16 offset:30240\n" \
  "ds_read_b64_tr_b16 a[238:239], v16 offset:32128\n" \
  "ds_read_b64_tr_b16 a[240:241], v16 offset:64\n" \
  "ds_read_b64_tr_b16 a[242:243], v16 offset:1952\n" \
  "ds_read_b64_tr_b16 a[244:245], v16 offset:30272\n" \
  "ds_read_b64_tr_b16 a[246:247], v16 offset:32160\n" \
  "ds_read_b64_tr_b16 a[248:249], v16 offset:96\n" \
  "ds_read_b64_tr_b16 a[250:251], v16 offset:1984\n" \
  "ds_read_b64_tr_b16 a[252:253], v16 offset:30304\n" \
  "ds_read_b64_tr_b16 a[254:255], v16 offset:32192\n" \
  "ds_read_b64_tr_b16 v[64:65], v16 offset:128\n" \
  "ds_read_b64_tr_b16 v[66:67], v16 offset:2016\n" \
  "ds_read_b64_tr_b16 v[68:69], v16 offset:30336\n" \
  "ds_read_b64_tr_b16 v[70:71], v16 offset:32224\n" \
  "ds_read_b64_tr_b16 v[72:73], v16 offset:160\n" \
  "ds_read_b64_tr_b16 v[74:75], v16 offset:2048\n" \
  "ds_read_b64_tr_b16 v[76:77], v16 offset:30368\n" \
  "ds_read_b64_tr_b16 v[78:79], v16 offset:32256\n" \
  "ds_read_b64_tr_b16 v[80:81], v16 offset:192\n" \
  "ds_read_b64_tr_b16 v[82:83], v16 offset:2080\n" \
  "ds_read_b64_tr_b16 v[84:85], v16 offset:30400\n" \
  "ds_read_b64_tr_b16 v[86:87], v16 offset:32288\n" \
  "v_xor_b32 v9, 0x10000, v9\n" \
  "v_xor_b32 v10, 0x10000, v10\n" \
  "v_xor_b32 v11, 0x10000, v11\n" \
  "v_xor_b32 v12, 0x10000, v12\n" \
  "v_xor_b32 v13, 0x10000, v13\n" \
  "v_xor_b32 v14, 0x10000, v14\n" \
  "v_xor_b32 v15, 0x10000, v15\n" \
  "v_xor_b32 v16, 0x10000, v16\n" \
  "v_xor_b32 v17, 0x10000, v17\n" \
  "v_xor_b32 v18, 0x10000, v18\n" \
  "s_mov_b32 s62, 0\n" \
  "buffer_load_dwordx4 a[0:3], v19, s[44:47], s62 offen nt\n" \
  "s_mov_b32 s62, 16384\n" \
  "buffer_load_dwordx4 a[4:7], v19, s[44:47], s62 offen nt\n" \
  "s_mov_b32 s62, 32768\n" \
  "buffer_load_dwordx4 a[8:11], v19, s[44:47], s62 offen nt\n" \
  "s_mov_b32 s62, 49152\n" \
  "buffer_load_dwordx4 a[12:15], v19, s[44:47], s62 offen nt\n" \
  "s_mov_b32 s62, 65536\n" \
  "buffer_load_dwordx4 a[16:19], v19, s[44:47], s62 offen nt\n" \
  "s_mov_b32 s62, 81920\n" \
  "buffer_load_dwordx4 a[20:23], v19, s[44:47], s62 offen nt\n" \
  "s_mov_b32 s62, 98304\n" \
  "buffer_load_dwordx4 a[24:27], v19, s[44:47], s62 offen nt\n" \
  "s_mov_b32 s62, 114688\n" \
  "buffer_load_dwordx4 a[28:31], v19, s[44:47], s62 offen nt\n" \
  "s_mov_b32 s62, 524288\n" \
  "buffer_load_dwordx4 a[32:35], v19, s[44:47], s62 offen nt\n" \
  "s_mov_b32 s62, 540672\n" \
  "buffer_load_dwordx4 a[36:39], v19, s[44:47], s62 offen nt\n" \
  "s_mov_b32 s62, 557056\n" \
  "buffer_load_dwordx4 a[40:43], v19, s[44:47], s62 offen nt\n" \
  "s_mov_b32 s62, 573440\n" \
  "buffer_load_dwordx4 a[44:47], v19, s[44:47], s62 offen nt\n" \
  "s_mov_b32 s62, 589824\n" \
  "buffer_load_dwordx4 a[48:51], v19, s[44:47], s62 offen nt\n" \
  "s_mov_b32 s62, 606208\n" \
  "buffer_load_dwordx4 a[52:55], v19, s[44:47], s62 offen nt\n" \
  "s_mov_b32 s62, 622592\n" \
  "buffer_load_dwordx4 a[56:59], v19, s[44:47], s62 offen nt\n" \
  "s_mov_b32 s62, 638976\n" \
  "buffer_load_dwordx4 a[60:63], v19, s[44:47], s62 offen nt\n" \
  "s_waitcnt vmcnt(44)\n" \
  "ds_write_b128 v9, v[88:91] offset:0\n" \
  "ds_write_b128 v10, v[92:95] offset:0\n" \
  "ds_write_b128 v11, v[96:99] offset:0\n" \
  "ds_write_b128 v12, v[100:103] offset:0\n" \
  "ds_write_b128 v13, v[104:107] offset:0\n" \
  "ds_write_b128 v14, v[108:111] offset:0\n" \
  "ds_write_b128 v15, v[112:115] offset:0\n" \
  "ds_write_b128 v9, v[116:119] offset:7552\n" \
  "ds_write_b128 v10, v[120:123] offset:7552\n" \
  "ds_write_b128 v11, v[124:127] offset:7552\n" \
  "ds_write_b128 v12, v[128:131] offset:7552\n" \
  "ds_write_b128 v13, v[132:135] offset:7552\n" \
  "ds_write_b128 v14, v[136:139] offset:7552\n" \
  "ds_write_b128 v15, v[140:143] offset:7552\n" \
  "s_waitcnt lgkmcnt(0)\n" \
  "s_barrier\n" \
  "ds_read_b64_tr_b16 v[88:89], v16 offset:0\n" \
  "ds_read_b64_tr_b16 v[90:91], v16 offset:1888\n" \
  "ds_read_b64_tr_b16 v[92:93], v16 offset:30208\n" \
  "ds_read_b64_tr_b16 v[94:95], v16 offset:32096\n" \
  "ds_read_b64_tr_b16 v[96:97], v16 offset:32\n" \
  "ds_read_b64_tr_b16 v[98:99], v16 offset:1920\n" \
  "ds_read_b64_tr_b16 v[100:101], v16 offset:30240\n" \
  "ds_read_b64_tr_b16 v[102:103], v16 offset:32128\n" \
  "ds_read_b64_tr_b16 v[104:105], v16 offset:64\n" \
  "ds_read_b64_tr_b16 v[106:107], v16 offset:1952\n" \
  "ds_read_b64_tr_b16 v[108:109], v16 offset:30272\n" \
  "ds_read_b64_tr_b16 v[110:111], v16 offset:32160\n" \
  "ds_read_b64_tr_b16 v[112:113], v16 offset:96\n" \
  "ds_read_b64_tr_b16 v[114:115], v16 offset:1984\n" \
  "ds_read_b64_tr_b16 v[116:117], v16 offset:30304\n" \
  "ds_read_b64_tr_b16 v[118:119], v16 offset:32192\n" \
  "ds_read_b64_tr_b16 v[120:121], v16 offset:128\n" \
  "ds_read_b64_tr_b16 v[122:123], v16 offset:2016\n" \
  "ds_read_b64_tr_b16 v[124:125], v16 offset:30336\n" \
  "ds_read_b64_tr_b16 v[126:127], v16 offset:32224\n" \
  "ds_read_b64_tr_b16 v[128:129], v16 offset:160\n" \
  "ds_read_b64_tr_b16 v[130:131], v16 offset:2048\n" \
  "ds_read_b64_tr_b16 v[132:133], v16 offset:30368\n" \
  "ds_read_b64_tr_b16 v[134:135], v16 offset:32256\n" \
  "ds_read_b64_tr_b16 v[136:137], v16 offset:192\n" \
  "ds_read_b64_tr_b16 v[138:139], v16 offset:2080\n" \
  "ds_read_b64_tr_b16 v[140:141], v16 offset:30400\n" \
  "ds_read_b64_tr_b16 v[142:143], v16 offset:32288\n" \
  "v_xor_b32 v9, 0x10000, v9\n" \
  "v_xor_b32 v10, 0x10000, v10\n" \
  "v_xor_b32 v11, 0x10000, v11\n" \
  "v_xor_b32 v12, 0x10000, v12\n" \
  "v_xor_b32 v13, 0x10000, v13\n" \
  "v_xor_b32 v14, 0x10000, v14\n" \
  "v_xor_b32 v15, 0x10000, v15\n" \
  "v_xor_b32 v16, 0x10000, v16\n" \
  "v_xor_b32 v17, 0x10000, v17\n" \
  "v_xor_b32 v18, 0x10000, v18\n" \
  "s_mov_b32 s62, 1048576\n" \
  "buffer_load_dwordx4 a[64:67], v19, s[44:47], s62 offen nt\n" \
  "s_mov_b32 s62, 1064960\n" \
  "buffer_load_dwordx4 a[68:71], v19, s[44:47], s62 offen nt\n" \
  "s_mov_b32 s62, 1081344\n" \
  "buffer_load_dwordx4 a[72:75], v19, s[44:47], s62 offen nt\n" \
  "s_mov_b32 s62, 1097728\n" \
  "buffer_load_dwordx4 a[76:79], v19, s[44:47], s62 offen nt\n" \
  "s_mov_b32 s62, 1114112\n" \
  "buffer_load_dwordx4 a[80:83], v19, s[44:47], s62 offen nt\n" \
  "s_mov_b32 s62, 1130496\n" \
  "buffer_load_dwordx4 a[84:87], v19, s[44:47], s62 offen nt\n" \
  "s_mov_b32 s62, 1146880\n" \
  "buffer_load_dwordx4 a[88:91], v19, s[44:47], s62 offen nt\n" \
  "s_mov_b32 s62, 1163264\n" \
  "buffer_load_dwordx4 a[92:95], v19, s[44:47], s62 offen nt\n" \
  "s_mov_b32 s62, 1572864\n" \
  "buffer_load_dwordx4 a[96:99], v19, s[44:47], s62 offen nt\n" \
  "s_mov_b32 s62, 1589248\n" \
  "buffer_load_dwordx4 a[100:103], v19, s[44:47], s62 offen nt\n" \
  "s_mov_b32 s62, 1605632\n" \
  "buffer_load_dwordx4 a[104:107], v19, s[44:47], s62 offen nt\n" \
  "s_mov_b32 s62, 1622016\n" \
  "buffer_load_dwordx4 a[108:111], v19, s[44:47], s62 offen nt\n" \
  "s_mov_b32 s62, 1638400\n" \
  "buffer_load_dwordx4 a[112:115], v19, s[44:47], s62 offen nt\n" \
  "s_mov_b32 s62, 1654784\n" \
  "buffer_load_dwordx4 a[116:119], v19, s[44:47], s62 offen nt\n" \
  "s_mov_b32 s62, 1671168\n" \
  "buffer_load_dwordx4 a[120:123], v19, s[44:47], s62 offen nt\n" \
  "s_mov_b32 s62, 1687552\n" \
  "buffer_load_dwordx4 a[124:127], v19, s[44:47], s62 offen nt\n" \
  "s_waitcnt vmcnt(46)\n" \
  "ds_write_b128 v9, v[144:147] offset:0\n" \
  "ds_write_b128 v10, v[148:151] offset:0\n" \
  "ds_write_b128 v11, v[152:155] offset:0\n" \
  "ds_write_b128 v12, v[156:159] offset:0\n" \
  "ds_write_b128 v13, v[160:163] offset:0\n" \
  "ds_write_b128 v14, v[164:167] offset:0\n" \
  "ds_write_b128 v15, v[168:171] offset:0\n" \
  "ds_write_b128 v9, v[172:175] offset:7552\n" \
  "ds_write_b128 v10, v[176:179] offset:7552\n" \
  "ds_write_b128 v11, v[180:183] offset:7552\n" \
  "ds_write_b128 v12, v[184:187] offset:7552\n" \
  "ds_write_b128 v13, v[188:191] offset:7552\n" \
  "ds_write_b128 v14, v[192:195] offset:7552\n" \
  "ds_write_b128 v15, v[196:199] offset:7552\n" \
  "s_waitcnt lgkmcnt(0)\n" \
  "s_barrier\n" \
  "ds_read_b64_tr_b16 v[144:145], v16 offset:0\n" \
  "ds_read_b64_tr_b16 v[146:147], v16 offset:1888\n" \
  "ds_read_b64_tr_b16 v[148:149], v16 offset:30208\n" \
  "ds_read_b64_tr_b16 v[150:151], v16 offset:32096\n" \
  "ds_read_b64_tr_b16 v[152:153], v16 offset:32\n" \
  "ds_read_b64_tr_b16 v[154:155], v16 offset:1920\n" \
  "ds_read_b64_tr_b16 v[156:157], v16 offset:30240\n" \
  "ds_read_b64_tr_b16 v[158:159], v16 offset:32128\n" \
  "ds_read_b64_tr_b16 v[160:161], v16 offset:64\n" \
  "ds_read_b64_tr_b16 v[162:163], v16 offset:1952\n" \
  "ds_read_b64_tr_b16 v[164:165], v16 offset:30272\n" \
  "ds_read_b64_tr_b16 v[166:167], v16 offset:32160\n" \
  "ds_read_b64_tr_b16 v[168:169], v16 offset:96\n" \
  "ds_read_b64_tr_b16 v[170:171], v16 offset:1984\n" \
  "ds_read_b64_tr_b16 v[172:173], v16 offset:30304\n" \
  "ds_read_b64_tr_b16 v[174:175], v16 offset:32192\n" \
  "ds_read_b64_tr_b16 v[176:177], v16 offset:128\n" \
  "ds_read_b64_tr_b16 v[178:179], v16 offset:2016\n" \
  "ds_read_b64_tr_b16 v[180:181], v16 offset:30336\n" \
  "ds_read_b64_tr_b16 v[182:183], v16 offset:32224\n" \
  "ds_read_b64_tr_b16 v[184:185], v16 offset:160\n" \
  "ds_read_b64_tr_b16 v[186:187], v16 offset:2048\n" \
  "ds_read_b64_tr_b16 v[188:189], v16 offset:30368\n" \
  "ds_read_b64_tr_b16 v[190:191], v16 offset:32256\n" \
  "ds_read_b64_tr_b16 v[192:193], v16 offset:192\n" \
  "ds_read_b64_tr_b16 v[194:195], v16 offset:2080\n" \
  "ds_read_b64_tr_b16 v[196:197], v16 offset:30400\n" \
  "ds_read_b64_tr_b16 v[198:199], v16 offset:32288\n" \
  "v_xor_b32 v9, 0x10000, v9\n" \
  "v_xor_b32 v10, 0x10000, v10\n" \
  "v_xor_b32 v11, 0x10000, v11\n" \
  "v_xor_b32 v12, 0x10000, v12\n" \
  "v_xor_b32 v13, 0x10000, v13\n" \
  "v_xor_b32 v14, 0x10000, v14\n" \
  "v_xor_b32 v15, 0x10000, v15\n" \
  "v_xor_b32 v16, 0x10000, v16\n" \
  "v_xor_b32 v17, 0x10000, v17\n" \
  "v_xor_b32 v18, 0x10000, v18\n" \
  "s_mov_b32 s62, 2097152\n" \
  "buffer_load_dwordx4 a[128:131], v19, s[44:47], s62 offen nt\n" \
  "s_mov_b32 s62, 2113536\n" \
  "buffer_load_dwordx4 a[132:135], v19, s[44:47], s62 offen nt\n" \
  "s_mov_b32 s62, 2129920\n" \
  "buffer_load_dwordx4 a[136:139], v19, s[44:47], s62 offen nt\n" \
  "s_mov_b32 s62, 2146304\n" \
  "buffer_load_dwordx4 a[140:143], v19, s[44:47], s62 offen nt\n" \
  "s_mov_b32 s62, 2162688\n" \
  "buffer_load_dwordx4 a[144:147], v19, s[44:47], s62 offen nt\n" \
  "s_mov_b32 s62, 2179072\n" \
  "buffer_load_dwordx4 a[148:151], v19, s[44:47], s62 offen nt\n" \
  "s_mov_b32 s62, 2195456\n" \
  "buffer_load_dwordx4 a[152:155], v19, s[44:47], s62 offen nt\n" \
  "s_mov_b32 s62, 2211840\n" \
  "buffer_load_dwordx4 a[156:159], v19, s[44:47], s62 offen nt\n" \
  "s_mov_b32 s62, 2621440\n" \
  "buffer_load_dwordx4 a[160:163], v19, s[44:47], s62 offen nt\n" \
  "s_mov_b32 s62, 2637824\n" \
  "buffer_load_dwordx4 a[164:167], v19, s[44:47], s62 offen nt\n" \
  "s_mov_b32 s62, 2654208\n" \
  "buffer_load_dwordx4 a[168:171], v19, s[44:47], s62 offen nt\n" \
  "s_mov_b32 s62, 2670592\n" \
  "buffer_load_dwordx4 a[172:175], v19, s[44:47], s62 offen nt\n" \
  "s_mov_b32 s62, 2686976\n" \
  "buffer_load_dwordx4 a[176:179], v19, s[44:47], s62 offen nt\n" \
  "s_mov_b32 s62, 2703360\n" \
  "buffer_load_dwordx4 a[180:183], v19, s[44:47], s62 offen nt\n" \
  "s_mov_b32 s62, 2719744\n" \
  "buffer_load_dwordx4 a[184:187], v19, s[44:47], s62 offen nt\n" \
  "s_mov_b32 s62, 2736128\n" \
  "buffer_load_dwordx4 a[188:191], v19, s[44:47], s62 offen nt\n" \
  "s_waitcnt vmcnt(48)\n" \
  "ds_write_b128 v9, v[200:203] offset:0\n" \
  "ds_write_b128 v10, v[204:207] offset:0\n" \
  "ds_write_b128 v11, v[208:211] offset:0\n" \
  "ds_write_b128 v12, v[212:215] offset:0\n" \
  "ds_write_b128 v13, v[216:219] offset:0\n" \
  "ds_write_b128 v14, v[220:223] offset:0\n" \
  "ds_write_b128 v15, v[224:227] offset:0\n" \
  "ds_write_b128 v9, v[228:231] offset:7552\n" \
  "ds_write_b128 v10, v[232:235] offset:7552\n" \
  "ds_write_b128 v11, v[236:239] offset:7552\n" \
  "ds_write_b128 v12, v[240:243] offset:7552\n" \
  "ds_write_b128 v13, v[244:247] offset:7552\n" \
  "ds_write_b128 v14, v[248:251] offset:7552\n" \
  "ds_write_b128 v15, v[252:255] offset:7552\n" \
  "s_waitcnt lgkmcnt(0)\n" \
  "s_barrier\n" \
  "ds_read_b64_tr_b16 v[200:201], v16 offset:0\n" \
  "ds_read_b64_tr_b16 v[202:203], v16 offset:1888\n" \
  "ds_read_b64_tr_b16 v[204:205], v16 offset:30208\n" \
  "ds_read_b64_tr_b16 v[206:207], v16 offset:32096\n" \
  "ds_read_b64_tr_b16 v[208:209], v16 offset:32\n" \
  "ds_read_b64_tr_b16 v[210:211], v16 offset:1920\n" \
  "ds_read_b64_tr_b16 v[212:213], v16 offset:30240\n" \
  "ds_read_b64_tr_b16 v[214:215], v16 offset:32128\n" \
  "ds_read_b64_tr_b16 v[216:217], v16 offset:64\n" \
  "ds_read_b64_tr_b16 v[218:219], v16 offset:1952\n" \
  "ds_read_b64_tr_b16 v[220:221], v16 offset:30272\n" \
  "ds_read_b64_tr_b16 v[222:223], v16 offset:32160\n" \
  "ds_read_b64_tr_b16 v[224:225], v16 offset:96\n" \
  "ds_read_b64_tr_b16 v[226:227], v16 offset:1984\n" \
  "ds_read_b64_tr_b16 v[228:229], v16 offset:30304\n" \
  "ds_read_b64_tr_b16 v[230:231], v16 offset:32192\n" \
  "ds_read_b64_tr_b16 v[232:233], v16 offset:128\n" \
  "ds_read_b64_tr_b16 v[234:235], v16 offset:2016\n" \
  "ds_read_b64_tr_b16 v[236:237], v16 offset:30336\n" \
  "ds_read_b64_tr_b16 v[238:239], v16 offset:32224\n" \
  "ds_read_b64_tr_b16 v[240:241], v16 offset:160\n" \
  "ds_read_b64_tr_b16 v[242:243], v16 offset:2048\n" \
  "ds_read_b64_tr_b16 v[244:245], v16 offset:30368\n" \
  "ds_read_b64_tr_b16 v[246:247], v16 offset:32256\n" \
  "ds_read_b64_tr_b16 v[248:249], v16 offset:192\n" \
  "ds_read_b64_tr_b16 v[250:251], v16 offset:2080\n" \
  "ds_read_b64_tr_b16 v[252:253], v16 offset:30400\n" \
  "ds_read_b64_tr_b16 v[254:255], v16 offset:32288\n" \
  "v_xor_b32 v9, 0x10000, v9\n" \
  "v_xor_b32 v10, 0x10000, v10\n" \
  "v_xor_b32 v11, 0x10000, v11\n" \
  "v_xor_b32 v12, 0x10000, v12\n" \
  "v_xor_b32 v13, 0x10000, v13\n" \
  "v_xor_b32 v14, 0x10000, v14\n" \
  "v_xor_b32 v15, 0x10000, v15\n" \
  "v_xor_b32 v16, 0x10000, v16\n" \
  "v_xor_b32 v17, 0x10000, v17\n" \
  "v_xor_b32 v18, 0x10000, v18\n" \
  "s_mov_b32 s62, 3145728\n" \
  "buffer_load_dwordx4 a[192:195], v19, s[44:47], s62 offen nt\n" \
  "s_mov_b32 s62, 3162112\n" \
  "buffer_load_dwordx4 a[196:199], v19, s[44:47], s62 offen nt\n" \
  "s_mov_b32 s62, 3178496\n" \
  "buffer_load_dwordx4 a[200:203], v19, s[44:47], s62 offen nt\n" \
  "s_mov_b32 s62, 3194880\n" \
  "buffer_load_dwordx4 a[204:207], v19, s[44:47], s62 offen nt\n" \
  "s_mov_b32 s62, 3211264\n" \
  "buffer_load_dwordx4 a[208:211], v19, s[44:47], s62 offen nt\n" \
  "s_mov_b32 s62, 3227648\n" \
  "buffer_load_dwordx4 a[212:215], v19, s[44:47], s62 offen nt\n" \
  "s_mov_b32 s62, 3244032\n" \
  "buffer_load_dwordx4 a[216:219], v19, s[44:47], s62 offen nt\n" \
  "s_mov_b32 s62, 3260416\n" \
  "buffer_load_dwordx4 a[220:223], v19, s[44:47], s62 offen nt\n" \
  "s_waitcnt vmcnt(48)\n" \
  "ds_write_b128 v17, a[0:3] offset:0\n" \
  "ds_write_b128 v17, a[4:7] offset:1120\n" \
  "ds_write_b128 v17, a[8:11] offset:2240\n" \
  "ds_write_b128 v17, a[12:15] offset:3360\n" \
  "ds_write_b128 v17, a[16:19] offset:4480\n" \
  "ds_write_b128 v17, a[20:23] offset:5600\n" \
  "ds_write_b128 v17, a[24:27] offset:6720\n" \
  "ds_write_b128 v17, a[28:31] offset:7840\n" \
  "s_waitcnt lgkmcnt(0)\n" \
  "s_barrier\n" \
  "ds_read_b64_tr_b16 a[0:1], v18 offset:0\n" \
  "ds_read_b64_tr_b16 a[2:3], v18 offset:1120\n" \
  "ds_read_b64_tr_b16 a[4:5], v18 offset:17920\n" \
  "ds_read_b64_tr_b16 a[6:7], v18 offset:19040\n" \
  "ds_read_b64_tr_b16 a[8:9], v18 offset:32\n" \
  "ds_read_b64_tr_b16 a[10:11], v18 offset:1152\n" \
  "ds_read_b64_tr_b16 a[12:13], v18 offset:17952\n" \
  "ds_read_b64_tr_b16 a[14:15], v18 offset:19072\n" \
  "ds_read_b64_tr_b16 a[16:17], v18 offset:64\n" \
  "ds_read_b64_tr_b16 a[18:19], v18 offset:1184\n" \
  "ds_read_b64_tr_b16 a[20:21], v18 offset:17984\n" \
  "ds_read_b64_tr_b16 a[22:23], v18 offset:19104\n" \
  "ds_read_b64_tr_b16 a[24:25], v18 offset:96\n" \
  "ds_read_b64_tr_b16 a[26:27], v18 offset:1216\n" \
  "ds_read_b64_tr_b16 a[28:29], v18 offset:18016\n" \
  "ds_read_b64_tr_b16 a[30:31], v18 offset:19136\n" \
  "v_xor_b32 v9, 0x10000, v9\n" \
  "v_xor_b32 v10, 0x10000, v10\n" \
  "v_xor_b32 v11, 0x10000, v11\n" \
  "v_xor_b32 v12, 0x10000, v12\n" \
  "v_xor_b32 v13, 0x10000, v13\n" \
  "v_xor_b32 v14, 0x10000, v14\n" \
  "v_xor_b32 v15, 0x10000, v15\n" \
  "v_xor_b32 v16, 0x10000, v16\n" \
  "v_xor_b32 v17, 0x10000, v17\n" \
  "v_xor_b32 v18, 0x10000, v18\n" \
  "s_waitcnt vmcnt(40)\n" \
  "ds_write_b128 v17, a[32:35] offset:0\n" \
  "ds_write_b128 v17, a[36:39] offset:1120\n" \
  "ds_write_b128 v17, a[40:43] offset:2240\n" \
  "ds_write_b128 v17, a[44:47] offset:3360\n" \
  "ds_write_b128 v17, a[48:51] offset:4480\n" \
  "ds_write_b128 v17, a[52:55] offset:5600\n" \
  "ds_write_b128 v17, a[56:59] offset:6720\n" \
  "ds_write_b128 v17, a[60:63] offset:7840\n" \
  "s_waitcnt lgkmcnt(0)\n" \
  "s_barrier\n" \
  "ds_read_b64_tr_b16 a[32:33], v18 offset:0\n" \
  "ds_read_b64_tr_b16 a[34:35], v18 offset:1120\n" \
  "ds_read_b64_tr_b16 a[36:37], v18 offset:17920\n" \
  "ds_read_b64_tr_b16 a[38:39], v18 offset:19040\n" \
  "ds_read_b64_tr_b16 a[40:41], v18 offset:32\n" \
  "ds_read_b64_tr_b16 a[42:43], v18 offset:1152\n" \
  "ds_read_b64_tr_b16 a[44:45], v18 offset:17952\n" \
  "ds_read_b64_tr_b16 a[46:47], v18 offset:19072\n" \
  "ds_read_b64_tr_b16 a[48:49], v18 offset:64\n" \
  "ds_read_b64_tr_b16 a[50:51], v18 offset:1184\n" \
  "ds_read_b64_tr_b16 a[52:53], v18 offset:17984\n" \
  "ds_read_b64_tr_b16 a[54:55], v18 offset:19104\n" \
  "ds_read_b64_tr_b16 a[56:57], v18 offset:96\n" \
  "ds_read_b64_tr_b16 a[58:59], v18 offset:1216\n" \
  "ds_read_b64_tr_b16 a[60:61], v18 offset:18016\n" \
  "ds_read_b64_tr_b16 a[62:63], v18 offset:19136\n" \
  "v_xor_b32 v9, 0x10000, v9\n" \
  "v_xor_b32 v10, 0x10000, v10\n" \
  "v_xor_b32 v11, 0x10000, v11\n" \
  "v_xor_b32 v12, 0x10000, v12\n" \
  "v_xor_b32 v13, 0x10000, v13\n" \
  "v_xor_b32 v14, 0x10000, v14\n" \
  "v_xor_b32 v15, 0x10000, v15\n" \
  "v_xor_b32 v16, 0x10000, v16\n" \
  "v_xor_b32 v17, 0x10000, v17\n" \
  "v_xor_b32 v18, 0x10000, v18\n" \
  "s_waitcnt vmcnt(32)\n" \
  "ds_write_b128 v17, a[64:67] offset:0\n" \
  "ds_write_b128 v17, a[68:71] offset:1120\n" \
  "ds_write_b128 v17, a[72:75] offset:2240\n" \
  "ds_write_b128 v17, a[76:79] offset:3360\n" \
  "ds_write_b128 v17, a[80:83] offset:4480\n" \
  "ds_write_b128 v17, a[84:87] offset:5600\n" \
  "ds_write_b128 v17, a[88:91] offset:6720\n" \
  "ds_write_b128 v17, a[92:95] offset:7840\n" \
  "s_waitcnt lgkmcnt(0)\n" \
  "s_barrier\n" \
  "ds_read_b64_tr_b16 a[64:65], v18 offset:0\n" \
  "ds_read_b64_tr_b16 a[66:67], v18 offset:1120\n" \
  "ds_read_b64_tr_b16 a[68:69], v18 offset:17920\n" \
  "ds_read_b64_tr_b16 a[70:71], v18 offset:19040\n" \
  "ds_read_b64_tr_b16 a[72:73], v18 offset:32\n" \
  "ds_read_b64_tr_b16 a[74:75], v18 offset:1152\n" \
  "ds_read_b64_tr_b16 a[76:77], v18 offset:17952\n" \
  "ds_read_b64_tr_b16 a[78:79], v18 offset:19072\n" \
  "ds_read_b64_tr_b16 a[80:81], v18 offset:64\n" \
  "ds_read_b64_tr_b16 a[82:83], v18 offset:1184\n" \
  "ds_read_b64_tr_b16 a[84:85], v18 offset:17984\n" \
  "ds_read_b64_tr_b16 a[86:87], v18 offset:19104\n" \
  "ds_read_b64_tr_b16 a[88:89], v18 offset:96\n" \
  "ds_read_b64_tr_b16 a[90:91], v18 offset:1216\n" \
  "ds_read_b64_tr_b16 a[92:93], v18 offset:18016\n" \
  "ds_read_b64_tr_b16 a[94:95], v18 offset:19136\n" \
  "v_xor_b32 v9, 0x10000, v9\n" \
  "v_xor_b32 v10, 0x10000, v10\n" \
  "v_xor_b32 v11, 0x10000, v11\n" \
  "v_xor_b32 v12, 0x10000, v12\n" \
  "v_xor_b32 v13, 0x10000, v13\n" \
  "v_xor_b32 v14, 0x10000, v14\n" \
  "v_xor_b32 v15, 0x10000, v15\n" \
  "v_xor_b32 v16, 0x10000, v16\n" \
  "v_xor_b32 v17, 0x10000, v17\n" \
  "v_xor_b32 v18, 0x10000, v18\n" \
  "s_waitcnt vmcnt(24)\n" \
  "ds_write_b128 v17, a[96:99] offset:0\n" \
  "ds_write_b128 v17, a[100:103] offset:1120\n" \
  "ds_write_b128 v17, a[104:107] offset:2240\n" \
  "ds_write_b128 v17, a[108:111] offset:3360\n" \
  "ds_write_b128 v17, a[112:115] offset:4480\n" \
  "ds_write_b128 v17, a[116:119] offset:5600\n" \
  "ds_write_b128 v17, a[120:123] offset:6720\n" \
  "ds_write_b128 v17, a[124:127] offset:7840\n" \
  "s_waitcnt lgkmcnt(0)\n" \
  "s_barrier\n" \
  "ds_read_b64_tr_b16 a[96:97], v18 offset:0\n" \
  "ds_read_b64_tr_b16 a[98:99], v18 offset:1120\n" \
  "ds_read_b64_tr_b16 a[100:101], v18 offset:17920\n" \
  "ds_read_b64_tr_b16 a[102:103], v18 offset:19040\n" \
  "ds_read_b64_tr_b16 a[104:105], v18 offset:32\n" \
  "ds_read_b64_tr_b16 a[106:107], v18 offset:1152\n" \
  "ds_read_b64_tr_b16 a[108:109], v18 offset:17952\n" \
  "ds_read_b64_tr_b16 a[110:111], v18 offset:19072\n" \
  "ds_read_b64_tr_b16 a[112:113], v18 offset:64\n" \
  "ds_read_b64_tr_b16 a[114:115], v18 offset:1184\n" \
  "ds_read_b64_tr_b16 a[116:117], v18 offset:17984\n" \
  "ds_read_b64_tr_b16 a[118:119], v18 offset:19104\n" \
  "ds_read_b64_tr_b16 a[120:121], v18 offset:96\n" \
  "ds_read_b64_tr_b16 a[122:123], v18 offset:1216\n" \
  "ds_read_b64_tr_b16 a[124:125], v18 offset:18016\n" \
  "ds_read_b64_tr_b16 a[126:127], v18 offset:19136\n" \
  "v_xor_b32 v9, 0x10000, v9\n" \
  "v_xor_b32 v10, 0x10000, v10\n" \
  "v_xor_b32 v11, 0x10000, v11\n" \
  "v_xor_b32 v12, 0x10000, v12\n" \
  "v_xor_b32 v13, 0x10000, v13\n" \
  "v_xor_b32 v14, 0x10000, v14\n" \
  "v_xor_b32 v15, 0x10000, v15\n" \
  "v_xor_b32 v16, 0x10000, v16\n" \
  "v_xor_b32 v17, 0x10000, v17\n" \
  "v_xor_b32 v18, 0x10000, v18\n" \
  "s_waitcnt vmcnt(16)\n" \
  "ds_write_b128 v17, a[128:131] offset:0\n" \
  "ds_write_b128 v17, a[132:135] offset:1120\n" \
  "ds_write_b128 v17, a[136:139] offset:2240\n" \
  "ds_write_b128 v17, a[140:143] offset:3360\n" \
  "ds_write_b128 v17, a[144:147] offset:4480\n" \
  "ds_write_b128 v17, a[148:151] offset:5600\n" \
  "ds_write_b128 v17, a[152:155] offset:6720\n" \
  "ds_write_b128 v17, a[156:159] offset:7840\n" \
  "s_waitcnt lgkmcnt(0)\n" \
  "s_barrier\n" \
  "ds_read_b64_tr_b16 a[128:129], v18 offset:0\n" \
  "ds_read_b64_tr_b16 a[130:131], v18 offset:1120\n" \
  "ds_read_b64_tr_b16 a[132:133], v18 offset:17920\n" \
  "ds_read_b64_tr_b16 a[134:135], v18 offset:19040\n" \
  "ds_read_b64_tr_b16 a[136:137], v18 offset:32\n" \
  "ds_read_b64_tr_b16 a[138:139], v18 offset:1152\n" \
  "ds_read_b64_tr_b16 a[140:141], v18 offset:17952\n" \
  "ds_read_b64_tr_b16 a[142:143], v18 offset:19072\n" \
  "ds_read_b64_tr_b16 a[144:145], v18 offset:64\n" \
  "ds_read_b64_tr_b16 a[146:147], v18 offset:1184\n" \
  "ds_read_b64_tr_b16 a[148:149], v18 offset:17984\n" \
  "ds_read_b64_tr_b16 a[150:151], v18 offset:19104\n" \
  "ds_read_b64_tr_b16 a[152:153], v18 offset:96\n" \
  "ds_read_b64_tr_b16 a[154:155], v18 offset:1216\n" \
  "ds_read_b64_tr_b16 a[156:157], v18 offset:18016\n" \
  "ds_read_b64_tr_b16 a[158:159], v18 offset:19136\n" \
  "v_xor_b32 v9, 0x10000, v9\n" \
  "v_xor_b32 v10, 0x10000, v10\n" \
  "v_xor_b32 v11, 0x10000, v11\n" \
  "v_xor_b32 v12, 0x10000, v12\n" \
  "v_xor_b32 v13, 0x10000, v13\n" \
  "v_xor_b32 v14, 0x10000, v14\n" \
  "v_xor_b32 v15, 0x10000, v15\n" \
  "v_xor_b32 v16, 0x10000, v16\n" \
  "v_xor_b32 v17, 0x10000, v17\n" \
  "v_xor_b32 v18, 0x10000, v18\n" \
  "s_waitcnt vmcnt(8)\n" \
  "ds_write_b128 v17, a[160:163] offset:0\n" \
  "ds_write_b128 v17, a[164:167] offset:1120\n" \
  "ds_write_b128 v17, a[168:171] offset:2240\n" \
  "ds_write_b128 v17, a[172:175] offset:3360\n" \
  "ds_write_b128 v17, a[176:179] offset:4480\n" \
  "ds_write_b128 v17, a[180:183] offset:5600\n" \
  "ds_write_b128 v17, a[184:187] offset:6720\n" \
  "ds_write_b128 v17, a[188:191] offset:7840\n" \
  "s_waitcnt lgkmcnt(0)\n" \
  "s_barrier\n" \
  "ds_read_b64_tr_b16 a[160:161], v18 offset:0\n" \
  "ds_read_b64_tr_b16 a[162:163], v18 offset:1120\n" \
  "ds_read_b64_tr_b16 a[164:165], v18 offset:17920\n" \
  "ds_read_b64_tr_b16 a[166:167], v18 offset:19040\n" \
  "ds_read_b64_tr_b16 a[168:169], v18 offset:32\n" \
  "ds_read_b64_tr_b16 a[170:171], v18 offset:1152\n" \
  "ds_read_b64_tr_b16 a[172:173], v18 offset:17952\n" \
  "ds_read_b64_tr_b16 a[174:175], v18 offset:19072\n" \
  "ds_read_b64_tr_b16 a[176:177], v18 offset:64\n" \
  "ds_read_b64_tr_b16 a[178:179], v18 offset:1184\n" \
  "ds_read_b64_tr_b16 a[180:181], v18 offset:17984\n" \
  "ds_read_b64_tr_b16 a[182:183], v18 offset:19104\n" \
  "ds_read_b64_tr_b16 a[184:185], v18 offset:96\n" \
  "ds_read_b64_tr_b16 a[186:187], v18 offset:1216\n" \
  "ds_read_b64_tr_b16 a[188:189], v18 offset:18016\n" \
  "ds_read_b64_tr_b16 a[190:191], v18 offset:19136\n" \
  "v_xor_b32 v9, 0x10000, v9\n" \
  "v_xor_b32 v10, 0x10000, v10\n" \
  "v_xor_b32 v11, 0x10000, v11\n" \
  "v_xor_b32 v12, 0x10000, v12\n" \
  "v_xor_b32 v13, 0x10000, v13\n" \
  "v_xor_b32 v14, 0x10000, v14\n" \
  "v_xor_b32 v15, 0x10000, v15\n" \
  "v_xor_b32 v16, 0x10000, v16\n" \
  "v_xor_b32 v17, 0x10000, v17\n" \
  "v_xor_b32 v18, 0x10000, v18\n" \
  "s_waitcnt vmcnt(0)\n" \
  "ds_write_b128 v17, a[192:195] offset:0\n" \
  "ds_write_b128 v17, a[196:199] offset:1120\n" \
  "ds_write_b128 v17, a[200:203] offset:2240\n" \
  "ds_write_b128 v17, a[204:207] offset:3360\n" \
  "ds_write_b128 v17, a[208:211] offset:4480\n" \
  "ds_write_b128 v17, a[212:215] offset:5600\n" \
  "ds_write_b128 v17, a[216:219] offset:6720\n" \
  "ds_write_b128 v17, a[220:223] offset:7840\n" \
  "s_waitcnt lgkmcnt(0)\n" \
  "s_barrier\n" \
  "ds_read_b64_tr_b16 a[192:193], v18 offset:0\n" \
  "ds_read_b64_tr_b16 a[194:195], v18 offset:1120\n" \
  "ds_read_b64_tr_b16 a[196:197], v18 offset:17920\n" \
  "ds_read_b64_tr_b16 a[198:199], v18 offset:19040\n" \
  "ds_read_b64_tr_b16 a[200:201], v18 offset:32\n" \
  "ds_read_b64_tr_b16 a[202:203], v18 offset:1152\n" \
  "ds_read_b64_tr_b16 a[204:205], v18 offset:17952\n" \
  "ds_read_b64_tr_b16 a[206:207], v18 offset:19072\n" \
  "ds_read_b64_tr_b16 a[208:209], v18 offset:64\n" \
  "ds_read_b64_tr_b16 a[210:211], v18 offset:1184\n" \
  "ds_read_b64_tr_b16 a[212:213], v18 offset:17984\n" \
  "ds_read_b64_tr_b16 a[214:215], v18 offset:19104\n" \
  "ds_read_b64_tr_b16 a[216:217], v18 offset:96\n" \
  "ds_read_b64_tr_b16 a[218:219], v18 offset:1216\n" \
  "ds_read_b64_tr_b16 a[220:221], v18 offset:18016\n" \
  "ds_read_b64_tr_b16 a[222:223], v18 offset:19136\n" \
  "v_xor_b32 v9, 0x10000, v9\n" \
  "v_xor_b32 v10, 0x10000, v10\n" \
  "v_xor_b32 v11, 0x10000, v11\n" \
  "v_xor_b32 v12, 0x10000, v12\n" \
  "v_xor_b32 v13, 0x10000, v13\n" \
  "v_xor_b32 v14, 0x10000, v14\n" \
  "v_xor_b32 v15, 0x10000, v15\n" \
  "v_xor_b32 v16, 0x10000, v16\n" \
  "v_xor_b32 v17, 0x10000, v17\n" \
  "v_xor_b32 v18, 0x10000, v18\n" \
  "Lldj_%=:\n" \
  "s_nop 1\n" \
  "s_waitcnt lgkmcnt(0)\n" \
  "s_barrier\n" \
  "v_and_b32 v32, 15, v0\n" \
  "v_lshrrev_b32 v33, 4, v0\n" \
  "v_lshrrev_b32 v34, 1, v32\n" \
  "v_min_u32 v34, 6, v34\n" \
  "v_and_b32 v35, 1, v32\n" \
  "s_mul_i32 s63, s60, 28\n" \
  "v_mul_u32_u24 v36, 7, v33\n" \
  "v_add_u32 v36, s63, v36\n" \
  "v_add_u32 v37, v36, v34\n" \
  "s_movk_i32 s63, 0x700\n" \
  "v_mul_u32_u24 v40, s63, v35\n" \
  "v_lshl_add_u32 v1, v37, 4, v40\n" \
  "s_movk_i32 s63, 0x380\n" \
  "v_mul_u32_u24 v40, s63, v35\n" \
  "v_lshl_add_u32 v7, v37, 3, v40\n" \
  "v_lshrrev_b32 v41, 1, v32\n" \
  "v_and_b32 v41, 1, v41\n" \
  "v_lshlrev_b32 v42, 5, v33\n" \
  "v_lshl_add_u32 v42, v35, 4, v42\n" \
  "v_mul_u32_u24 v40, s63, v41\n" \
  "v_add_u32 v4, v42, v40\n" \
  "v_lshl_add_u32 v5, v41, 9, v42\n" \
  "v_add_u32 v5, 4096, v5\n" \
  "v_mov_b32 v6, 0x4444\n" \
  "v_mov_b32 v40, 0xeeee\n" \
  "v_cmp_eq_u32 vcc, 1, v35\n" \
  "s_nop 1\n" \
  "v_cndmask_b32 v6, v6, v40, vcc\n" \
  "v_cvt_f32_u32 v14, v35\n" \
  "s_lshl_b32 s63, s60, 6\n" \
  "s_add_u32 s63, s63, 1792\n" \
  "v_lshlrev_b32 v11, 4, v33\n" \
  "v_add_u32 v11, s63, v11\n" \
  "v_lshlrev_b32 v13, 4, v32\n" \
  "v_add_u32 v13, 1792, v13\n" \
  "s_mul_i32 s63, s60, 448\n" \
  "v_lshlrev_b32 v12, 2, v0\n" \
  "v_add_u32 v12, s63, v12\n" \
  "s_cmp_lg_u32 s77, 0\n" \
  "s_cbranch_scc1 LGmain_%=\n" \
  "v_lshrrev_b32 v40, 1, v32\n" \
  "v_lshlrev_b32 v41, 5, v33\n" \
  "v_lshl_add_u32 v41, v35, 4, v41\n" \
  "s_movk_i32 s63, 0x110\n" \
  "v_mul_u32_u24 v43, s63, v40\n" \
  "v_add_u32 v43, v43, v41\n" \
  "v_add_u32 v2, s87, v43\n" \
  "v_cmp_gt_u32 vcc, 6, v40\n" \
  "v_add_u32 v42, 2176, v2\n" \
  "s_nop 0\n" \
  "v_cndmask_b32 v3, v2, v42, vcc\n" \
  "v_lshrrev_b32 v9, 1, v41\n" \
  "v_and_b32 v42, 1, v40\n" \
  "v_lshl_add_u32 v9, v42, 6, v9\n" \
  "v_lshrrev_b32 v42, 1, v40\n" \
  "v_and_b32 v42, 1, v42\n" \
  "v_cvt_f32_u32 v15, v42\n" \
  "v_lshl_add_u32 v9, v42, 9, v9\n" \
  "s_lshl_b32 s63, s60, 7\n" \
  "s_add_u32 s63, s63, 4096\n" \
  "v_add_u32 v9, s63, v9\n" \
  "s_mov_b32 s20, 0xcccccccc\n" \
  "s_mov_b32 s21, 0xcccccccc\n" \
  "s_waitcnt vmcnt(0)\n" \
  "v_readfirstlane_b32 s80, v60\n" \
  "s_nop 3\n" \
  "s_mul_i32 s80, s80, 0x9e3779b1\n" \
  "s_add_u32 s80, s80, 0x7f4a7c15\n" \
  "v_lshlrev_b32 v16, 16, v46\n" \
  "v_and_b32 v17, 0xffff0000, v46\n" \
  "v_lshlrev_b32 v18, 16, v47\n" \
  "v_and_b32 v19, 0xffff0000, v47\n" \
  "v_lshlrev_b32 v20, 16, v48\n" \
  "v_and_b32 v21, 0xffff0000, v48\n" \
  "v_lshlrev_b32 v22, 16, v49\n" \
  "v_and_b32 v23, 0xffff0000, v49\n" \
  "s_mov_b32 exec_lo, 0x15551555\n" \
  "s_mov_b32 exec_hi, 0x15551555\n" \
  "ds_write_b128 v1, v[16:19] offset:6144\n" \
  "ds_write_b128 v1, v[20:23] offset:7936\n" \
  "s_mov_b64 exec, -1\n" \
  "s_waitcnt lgkmcnt(0)\n" \
  "s_lshl_b32 s63, s60, 7\n" \
  "v_and_b32 v8, 1, v0\n" \
  "v_lshlrev_b32 v8, 1, v8\n" \
  "v_and_b32 v40, 12, v0\n" \
  "v_add_u32 v8, v8, v40\n" \
  "v_and_b32 v40, 2, v0\n" \
  "v_lshl_add_u32 v8, v40, 4, v8\n" \
  "v_and_b32 v40, 16, v0\n" \
  "v_lshl_add_u32 v8, v40, 2, v8\n" \
  "v_and_b32 v40, 32, v0\n" \
  "v_lshrrev_b32 v40, 1, v40\n" \
  "v_add_u32 v8, v8, v40\n" \
  "v_add_u32 v8, s63, v8\n" \
  "s_mov_b32 s73, 1\n" \
  "v_mov_b32 v56, 1.0\n" \
  "v_mov_b32 v57, 1.0\n" \
  "v_mov_b32 v58, 1.0\n" \
  "v_mov_b32 v59, 1.0\n" \
  "s_mov_b32 s79, 9728\n" \
  "s_nop 0\n" \
  "s_call_b64 s[64:65], LLNQ_%=\n" \
  "s_nop 1\n" \
  "s_call_b64 s[64:65], LQOUT_%=\n" \
  "s_nop 1\n" \
  "s_lshl_b32 s63, s61, 2\n" \
  "s_add_u32 s63, s63, s60\n" \
  "s_mul_i32 s67, s63, 3808\n" \
  "s_add_u32 s67, s67, 280576\n" \
  "s_add_u32 s67, s67, 256\n" \
  "s_movk_i32 s68, 272\n" \
  "s_mov_b32 s69, 0x3fff\n" \
  "s_mov_b32 s90, 1\n" \
  "s_call_b64 s[64:65], LCAN_%=\n" \
  "s_nop 1\n" \
  "s_lshl_b32 s63, s61, 2\n" \
  "s_add_u32 s63, s63, s60\n" \
  "s_mul_i32 s67, s63, 3808\n" \
  "s_add_u32 s67, s67, 280576\n" \
  "s_call_b64 s[64:65], LPG_%=\n" \
  "s_nop 1\n" \
  "s_mul_i32 s67, s61, 1792\n" \
  "s_add_u32 s67, s67, 65536\n" \
  "s_mov_b32 s68, 1\n" \
  "s_mov_b32 s79, 6144\n" \
  "s_call_b64 s[64:65], LGO_%=\n" \
  "s_nop 1\n" \
  "s_mov_b32 s67, 180224\n" \
  "s_mov_b32 s68, 0\n" \
  "s_call_b64 s[64:65], LGATH_%=\n" \
  "s_nop 1\n" \
  "s_mov_b32 s73, 0\n" \
  "s_call_b64 s[64:65], LRDY_%=\n" \
  "s_nop 1\n" \
  "s_lshl_b32 s63, s61, 2\n" \
  "s_add_u32 s63, s63, s60\n" \
  "s_mul_i32 s67, s63, 3808\n" \
  "s_add_u32 s67, s67, 280576\n" \
  "s_add_u32 s67, s67, 256\n" \
  "s_movk_i32 s68, 272\n" \
  "s_mov_b32 s69, 0x3fff\n" \
  "s_mov_b32 s90, 1\n" \
  "s_call_b64 s[64:65], LCAN_%=\n" \
  "s_nop 1\n" \
  "s_lshl_b32 s63, s61, 2\n" \
  "s_add_u32 s63, s63, s60\n" \
  "s_mul_i32 s67, s63, 3808\n" \
  "s_add_u32 s67, s67, 280576\n" \
  "s_call_b64 s[64:65], LPG_%=\n" \
  "s_nop 1\n" \
  "s_mul_i32 s67, s61, 1792\n" \
  "s_add_u32 s67, s67, 65536\n" \
  "s_mov_b32 s68, 1\n" \
  "s_mov_b32 s79, 6144\n" \
  "s_call_b64 s[64:65], LGO_%=\n" \
  "s_nop 1\n" \
  "s_mul_i32 s67, s60, 448\n" \
  "s_add_u32 s67, s67, 180224\n" \
  "s_movk_i32 s68, 1792\n" \
  "s_mov_b32 s69, 0x3fff\n" \
  "s_mov_b32 s90, 1\n" \
  "s_call_b64 s[64:65], LCAN_%=\n" \
  "s_nop 1\n" \
  "s_mov_b32 s67, 180224\n" \
  "s_mov_b32 s68, 0\n" \
  "s_call_b64 s[64:65], LGATH_%=\n" \
  "s_nop 1\n" \
  "s_mov_b32 s79, 6144\n" \
  "s_call_b64 s[64:65], LLNQ_%=\n" \
  "s_nop 1\n" \
  "s_lshl_b32 s67, s61, 10\n" \
  "s_add_u32 s67, s67, 32768\n" \
  "s_call_b64 s[64:65], LQOUT_%=\n" \
  "s_nop 1\n" \
  "s_lshl_b32 s63, s61, 2\n" \
  "s_add_u32 s63, s63, s60\n" \
  "s_mul_i32 s67, s63, 3808\n" \
  "s_add_u32 s67, s67, 524288\n" \
  "s_add_u32 s67, s67, 256\n" \
  "s_movk_i32 s68, 272\n" \
  "s_mov_b32 s69, 0x3fff\n" \
  "s_mov_b32 s90, 1\n" \
  "s_call_b64 s[64:65], LCAN_%=\n" \
  "s_nop 1\n" \
  "s_lshl_b32 s63, s61, 2\n" \
  "s_add_u32 s63, s63, s60\n" \
  "s_mul_i32 s67, s63, 3808\n" \
  "s_add_u32 s67, s67, 524288\n" \
  "s_call_b64 s[64:65], LPG_%=\n" \
  "s_nop 1\n" \
  "s_mul_i32 s67, s61, 1792\n" \
  "s_add_u32 s67, s67, 94208\n" \
  "s_mov_b32 s68, 1\n" \
  "s_mov_b32 s79, 7936\n" \
  "s_call_b64 s[64:65], LGO_%=\n" \
  "s_nop 1\n" \
  "s_mul_i32 s67, s60, 448\n" \
  "s_add_u32 s67, s67, 205312\n" \
  "s_movk_i32 s68, 1792\n" \
  "s_mov_b32 s69, 0x3fff\n" \
  "s_mov_b32 s90, 1\n" \
  "s_call_b64 s[64:65], LCAN_%=\n" \
  "s_nop 1\n" \
  "s_mov_b32 s67, 205312\n" \
  "s_mov_b32 s68, 0\n" \
  "s_call_b64 s[64:65], LGATH_%=\n" \
  "s_nop 1\n" \
  "s_mov_b32 s79, 7936\n" \
  "s_call_b64 s[64:65], LLNQ_%=\n" \
  "s_nop 1\n" \
  "s_lshl_b32 s67, s61, 10\n" \
  "s_add_u32 s67, s67, 49152\n" \
  "s_call_b64 s[64:65], LQOUT_%=\n" \
  "s_nop 1\n" \
  "s_lshl_b32 s63, s61, 2\n" \
  "s_add_u32 s63, s63, s60\n" \
  "s_mul_i32 s67, s63, 3808\n" \
  "s_add_u32 s67, s67, 768000\n" \
  "s_add_u32 s67, s67, 256\n" \
  "s_movk_i32 s68, 272\n" \
  "s_mov_b32 s69, 0x3fff\n" \
  "s_mov_b32 s90, 14\n" \
  "s_call_b64 s[64:65], LCAN_%=\n" \
  "s_nop 1\n" \
  "s_lshl_b32 s63, s61, 2\n" \
  "s_add_u32 s63, s63, s60\n" \
  "s_mul_i32 s67, s63, 3808\n" \
  "s_add_u32 s67, s67, 768000\n" \
  "s_call_b64 s[64:65], LPG_%=\n" \
  "s_nop 1\n" \
  "s_mul_i32 s67, s61, 1792\n" \
  "s_add_u32 s67, s67, 122880\n" \
  "s_mov_b32 s68, 1\n" \
  "s_mov_b32 s79, 6144\n" \
  "s_call_b64 s[64:65], LGO_%=\n" \
  "s_nop 1\n" \
  "s_lshl_b32 s63, s61, 2\n" \
  "s_add_u32 s63, s63, s60\n" \
  "s_mul_i32 s67, s63, 3808\n" \
  "s_add_u32 s67, s67, 1011712\n" \
  "s_add_u32 s67, s67, 256\n" \
  "s_movk_i32 s68, 272\n" \
  "s_mov_b32 s69, 0x3fff\n" \
  "s_mov_b32 s90, 1\n" \
  "s_call_b64 s[64:65], LCAN_%=\n" \
  "s_nop 1\n" \
  "s_lshl_b32 s63, s61, 2\n" \
  "s_add_u32 s63, s63, s60\n" \
  "s_mul_i32 s67, s63, 3808\n" \
  "s_add_u32 s67, s67, 1011712\n" \
  "s_call_b64 s[64:65], LPG_%=\n" \
  "s_nop 1\n" \
  "s_mul_i32 s67, s61, 1792\n" \
  "s_add_u32 s67, s67, 151552\n" \
  "s_mov_b32 s68, 1\n" \
  "s_mov_b32 s79, 7936\n" \
  "s_call_b64 s[64:65], LGO_%=\n" \
  "s_nop 1\n" \
  "s_branch Lsend_%=\n" \
  "LGmain_%=:\n" \
  "s_nop 1\n" \
  "v_lshrrev_b32 v40, 1, v33\n" \
  "v_cvt_f32_u32 v15, v40\n" \
  "v_lshlrev_b32 v9, 9, v40\n" \
  "v_and_b32 v41, 1, v33\n" \
  "v_lshl_add_u32 v9, v41, 5, v9\n" \
  "v_lshl_add_u32 v9, v32, 1, v9\n" \
  "s_lshl_b32 s63, s60, 6\n" \
  "s_add_u32 s63, s63, 4096\n" \
  "v_add_u32 v9, s63, v9\n" \
  "s_waitcnt vmcnt(0)\n" \
  "s_waitcnt lgkmcnt(0)\n" \
  "s_mov_b32 s73, 1\n" \
  "s_mul_i32 s67, s60, 448\n" \
  "s_add_u32 s67, s67, 65536\n" \
  "s_movk_i32 s68, 1792\n" \
  "s_mov_b32 s69, 0xffff\n" \
  "s_mov_b32 s90, 1\n" \
  "s_call_b64 s[64:65], LCAN_%=\n" \
  "s_nop 1\n" \
  "s_mov_b32 s67, 65536\n" \
  "s_mov_b32 s68, 1\n" \
  "s_call_b64 s[64:65], LGATH_%=\n" \
  "s_nop 1\n" \
  "v_mov_b32 v56, 1.0\n" \
  "v_mov_b32 v57, 1.0\n" \
  "v_mov_b32 v58, 1.0\n" \
  "v_mov_b32 v59, 1.0\n" \
  "s_mov_b32 s79, 7936\n" \
  "s_nop 0\n" \
  "s_call_b64 s[64:65], LLNQ_%=\n" \
  "s_nop 1\n" \
  "s_call_b64 s[64:65], LHB_%=\n" \
  "s_nop 1\n" \
  "s_mul_i32 s67, s61, 1792\n" \
  "s_add_u32 s67, s67, 180224\n" \
  "s_mov_b32 s68, 0\n" \
  "s_mov_b32 s79, 7936\n" \
  "s_call_b64 s[64:65], LGO_%=\n" \
  "s_nop 1\n" \
  "s_lshl_b32 s67, s61, 7\n" \
  "s_add_u32 s67, s67, 230400\n" \
  "s_lshl_b32 s68, s61, 6\n" \
  "s_add_u32 s68, s68, 0\n" \
  "s_lshl_b32 s67, s61, 7\n" \
  "s_lshl_b32 s68, s61, 6\n" \
  "s_cmp_eq_u32 s60, 0\n" \
  "s_cselect_b32 s63, 25088, 0\n" \
  "s_add_u32 s67, s67, s63\n" \
  "s_add_u32 s67, s67, 230400\n" \
  "s_cmp_eq_u32 s60, 0\n" \
  "s_cselect_b32 s63, 896, 0\n" \
  "s_add_u32 s68, s68, s63\n" \
  "s_call_b64 s[64:65], LFIN_%=\n" \
  "s_nop 1\n" \
  "s_mov_b32 s73, 0\n" \
  "s_call_b64 s[64:65], LRDY_%=\n" \
  "s_nop 1\n" \
  "s_mul_i32 s67, s60, 448\n" \
  "s_add_u32 s67, s67, 65536\n" \
  "s_movk_i32 s68, 1792\n" \
  "s_mov_b32 s69, 0xffff\n" \
  "s_mov_b32 s90, 1\n" \
  "s_call_b64 s[64:65], LCAN_%=\n" \
  "s_nop 1\n" \
  "s_mov_b32 s67, 65536\n" \
  "s_mov_b32 s68, 1\n" \
  "s_call_b64 s[64:65], LGATH_%=\n" \
  "s_nop 1\n" \
  "s_mov_b32 s79, 6144\n" \
  "s_call_b64 s[64:65], LLNQ_%=\n" \
  "s_nop 1\n" \
  "s_call_b64 s[64:65], LHB_%=\n" \
  "s_nop 1\n" \
  "s_mul_i32 s67, s61, 1792\n" \
  "s_add_u32 s67, s67, 180224\n" \
  "s_mov_b32 s68, 0\n" \
  "s_mov_b32 s79, 6144\n" \
  "s_call_b64 s[64:65], LGO_%=\n" \
  "s_nop 1\n" \
  "s_mul_i32 s67, s60, 448\n" \
  "s_add_u32 s67, s67, 94208\n" \
  "s_movk_i32 s68, 1792\n" \
  "s_mov_b32 s69, 0xffff\n" \
  "s_mov_b32 s90, 1\n" \
  "s_call_b64 s[64:65], LCAN_%=\n" \
  "s_nop 1\n" \
  "s_mov_b32 s67, 94208\n" \
  "s_mov_b32 s68, 1\n" \
  "s_call_b64 s[64:65], LGATH_%=\n" \
  "s_nop 1\n" \
  "s_mov_b32 s79, 6144\n" \
  "s_call_b64 s[64:65], LLNQ_%=\n" \
  "s_nop 1\n" \
  "s_call_b64 s[64:65], LHB_%=\n" \
  "s_nop 1\n" \
  "s_mul_i32 s67, s61, 1792\n" \
  "s_add_u32 s67, s67, 205312\n" \
  "s_mov_b32 s68, 0\n" \
  "s_mov_b32 s79, 6144\n" \
  "s_call_b64 s[64:65], LGO_%=\n" \
  "s_nop 1\n" \
  "s_mul_i32 s67, s60, 448\n" \
  "s_add_u32 s67, s67, 122880\n" \
  "s_movk_i32 s68, 1792\n" \
  "s_mov_b32 s69, 0xffff\n" \
  "s_mov_b32 s90, 16\n" \
  "s_call_b64 s[64:65], LCAN_%=\n" \
  "s_nop 1\n" \
  "s_mov_b32 s67, 122880\n" \
  "s_mov_b32 s68, 1\n" \
  "s_call_b64 s[64:65], LGATH_%=\n" \
  "s_nop 1\n" \
  "s_mov_b32 s79, 7936\n" \
  "s_call_b64 s[64:65], LLNQ_%=\n" \
  "s_nop 1\n" \
  "s_call_b64 s[64:65], LHB_%=\n" \
  "s_nop 1\n" \
  "s_mul_i32 s67, s61, 1792\n" \
  "s_add_u32 s67, s67, 230400\n" \
  "s_mov_b32 s68, 0\n" \
  "s_mov_b32 s79, 7936\n" \
  "s_call_b64 s[64:65], LGO_%=\n" \
  "s_nop 1\n" \
  "s_mul_i32 s67, s60, 448\n" \
  "s_add_u32 s67, s67, 151552\n" \
  "s_movk_i32 s68, 1792\n" \
  "s_mov_b32 s69, 0xffff\n" \
  "s_mov_b32 s90, 1\n" \
  "s_call_b64 s[64:65], LCAN_%=\n" \
  "s_nop 1\n" \
  "s_mov_b32 s67, 151552\n" \
  "s_mov_b32 s68, 1\n" \
  "s_call_b64 s[64:65], LGATH_%=\n" \
  "s_nop 1\n" \
  "s_mov_b32 s79, 7936\n" \
  "s_call_b64 s[64:65], LLNQ_%=\n" \
  "s_nop 1\n" \
  "s_call_b64 s[64:65], LHB_%=\n" \
  "s_nop 1\n" \
  "s_mul_i32 s67, s61, 1792\n" \
  "s_add_u32 s67, s67, 255488\n" \
  "s_mov_b32 s68, 0\n" \
  "s_mov_b32 s79, 7936\n" \
  "s_call_b64 s[64:65], LGO_%=\n" \
  "s_nop 1\n" \
  "s_lshl_b32 s67, s61, 7\n" \
  "s_add_u32 s67, s67, 255488\n" \
  "s_lshl_b32 s68, s61, 6\n" \
  "s_add_u32 s68, s68, 896\n" \
  "s_lshl_b32 s67, s61, 7\n" \
  "s_lshl_b32 s68, s61, 6\n" \
  "s_cmp_eq_u32 s60, 0\n" \
  "s_cselect_b32 s63, 25088, 0\n" \
  "s_add_u32 s67, s67, s63\n" \
  "s_add_u32 s67, s67, 230400\n" \
  "s_cmp_eq_u32 s60, 0\n" \
  "s_cselect_b32 s63, 896, 0\n" \
  "s_add_u32 s68, s68, s63\n" \
  "s_call_b64 s[64:65], LFIN_%=\n" \
  "s_nop 1\n" \
  "s_add_u32 s63, s61, s60\n" \
  "s_cmp_lg_u32 s63, 0\n" \
  "s_cbranch_scc1 Lep_%=\n" \
  "s_mov_b32 s63, 1255552\n" \
  "v_mov_b32 v61, s80\n" \
  "s_mov_b32 exec_lo, 0x1\n" \
  "s_mov_b32 exec_hi, 0x0\n" \
  "buffer_store_dword v61, off, s[40:43], s63 sc1\n" \
  "s_mov_b64 exec, -1\n" \
  "Lep_%=:\n" \
  "s_branch Lsend_%=\n" \
  "LGATH_%=:\n" \
  "s_nop 1\n" \
  "s_mul_i32 s49, s60, 448\n" \
  "v_lshlrev_b32 v61, 4, v0\n" \
  "v_cmp_lt_u32 vcc, 27, v0\n" \
  "v_add_u32 v62, 1344, v61\n" \
  "s_nop 0\n" \
  "v_cndmask_b32 v61, v61, v62, vcc\n" \
  "v_add_u32 v61, s49, v61\n" \
  "s_sub_u32 s49, s86, s49\n" \
  "v_and_b32 v62, 1, v0\n" \
  "v_mul_u32_u24 v62, 1344, v62\n" \
  "v_sub_u32 v63, v1, v62\n" \
  "v_add_u32 v63, s49, v63\n" \
  "s_mov_b32 exec_hi, 0x00ffffff\n" \
  "s_add_u32 s62, s67, 0\n" \
  "s_add_u32 m0, s86, 0\n" \
  "s_nop 0\n" \
  "buffer_load_dwordx4 v61, s[40:43], s62 offen offset:0 sc1 lds\n" \
  "s_add_u32 m0, s86, -2560\n" \
  "s_nop 0\n" \
  "buffer_load_dwordx4 v61, s[40:43], s62 offen offset:3584 sc1 lds\n" \
  "s_add_u32 s62, s67, 7168\n" \
  "s_add_u32 m0, s86, 2048\n" \
  "s_nop 0\n" \
  "buffer_load_dwordx4 v61, s[40:43], s62 offen offset:0 sc1 lds\n" \
  "s_add_u32 m0, s86, -512\n" \
  "s_nop 0\n" \
  "buffer_load_dwordx4 v61, s[40:43], s62 offen offset:3584 sc1 lds\n" \
  "s_add_u32 s62, s67, 14336\n" \
  "s_add_u32 m0, s86, 4096\n" \
  "s_nop 0\n" \
  "buffer_load_dwordx4 v61, s[40:43], s62 offen offset:0 sc1 lds\n" \
  "s_add_u32 m0, s86, 1536\n" \
  "s_nop 0\n" \
  "buffer_load_dwordx4 v61, s[40:43], s62 offen offset:3584 sc1 lds\n" \
  "s_add_u32 s62, s67, 21504\n" \
  "s_add_u32 m0, s86, 6144\n" \
  "s_nop 0\n" \
  "buffer_load_dwordx4 v61, s[40:43], s62 offen offset:0 sc1 lds\n" \
  "s_add_u32 m0, s86, 3584\n" \
  "s_nop 0\n" \
  "buffer_load_dwordx4 v61, s[40:43], s62 offen offset:3584 sc1 lds\n" \
  "s_mov_b32 exec_hi, -1\n" \
  "s_cmp_lg_u32 s73, 0\n" \
  "s_cbranch_scc1 LGATH_3_%=\n" \
  "s_sleep 12\n" \
  "s_mov_b32 exec_hi, 0x00ffffff\n" \
  "s_add_u32 s62, s67, 0\n" \
  "s_add_u32 m0, s86, 0\n" \
  "s_nop 0\n" \
  "buffer_load_dwordx4 v61, s[40:43], s62 offen offset:0 sc1 lds\n" \
  "s_add_u32 m0, s86, -2560\n" \
  "s_nop 0\n" \
  "buffer_load_dwordx4 v61, s[40:43], s62 offen offset:3584 sc1 lds\n" \
  "s_add_u32 s62, s67, 7168\n" \
  "s_add_u32 m0, s86, 2048\n" \
  "s_nop 0\n" \
  "buffer_load_dwordx4 v61, s[40:43], s62 offen offset:0 sc1 lds\n" \
  "s_add_u32 m0, s86, -512\n" \
  "s_nop 0\n" \
  "buffer_load_dwordx4 v61, s[40:43], s62 offen offset:3584 sc1 lds\n" \
  "s_add_u32 s62, s67, 14336\n" \
  "s_add_u32 m0, s86, 4096\n" \
  "s_nop 0\n" \
  "buffer_load_dwordx4 v61, s[40:43], s62 offen offset:0 sc1 lds\n" \
  "s_add_u32 m0, s86, 1536\n" \
  "s_nop 0\n" \
  "buffer_load_dwordx4 v61, s[40:43], s62 offen offset:3584 sc1 lds\n" \
  "s_add_u32 s62, s67, 21504\n" \
  "s_add_u32 m0, s86, 6144\n" \
  "s_nop 0\n" \
  "buffer_load_dwordx4 v61, s[40:43], s62 offen offset:0 sc1 lds\n" \
  "s_add_u32 m0, s86, 3584\n" \
  "s_nop 0\n" \
  "buffer_load_dwordx4 v61, s[40:43], s62 offen offset:3584 sc1 lds\n" \
  "s_mov_b32 exec_hi, -1\n" \
  "LGATH_1_%=:\n" \
  "s_waitcnt vmcnt(8)\n" \
  "LGATH_4_%=:\n" \
  "s_nop 1\n" \
  "ds_read_b128 v[24:27], v63 offset:0\n" \
  "ds_read_b128 v[28:31], v63 offset:1024\n" \
  "ds_read_b128 v[32:35], v63 offset:2048\n" \
  "ds_read_b128 v[36:39], v63 offset:3072\n" \
  "ds_read_b128 v[40:43], v63 offset:4096\n" \
  "ds_read_b128 v[44:47], v63 offset:5120\n" \
  "ds_read_b128 v[48:51], v63 offset:6144\n" \
  "ds_read_b128 v[52:55], v63 offset:7168\n" \
  "s_waitcnt lgkmcnt(6)\n" \
  "v_pk_add_f32 v[24:25], v[24:25], v[28:29]\n" \
  "v_pk_add_f32 v[26:27], v[26:27], v[30:31]\n" \
  "s_waitcnt lgkmcnt(4)\n" \
  "v_pk_add_f32 v[32:33], v[32:33], v[36:37]\n" \
  "v_pk_add_f32 v[34:35], v[34:35], v[38:39]\n" \
  "s_waitcnt lgkmcnt(2)\n" \
  "v_pk_add_f32 v[40:41], v[40:41], v[44:45]\n" \
  "v_pk_add_f32 v[42:43], v[42:43], v[46:47]\n" \
  "v_pk_add_f32 v[24:25], v[24:25], v[32:33]\n" \
  "v_pk_add_f32 v[26:27], v[26:27], v[34:35]\n" \
  "s_waitcnt lgkmcnt(0)\n" \
  "s_cmp_eq_u32 s68, 0\n" \
  "s_cbranch_scc1 LGATH_5_%=\n" \
  "v_pk_add_f32 v[48:49], v[48:49], v[52:53]\n" \
  "v_pk_add_f32 v[50:51], v[50:51], v[54:55]\n" \
  "LGATH_5_%=:\n" \
  "v_pk_add_f32 v[40:41], v[40:41], v[48:49]\n" \
  "v_pk_add_f32 v[42:43], v[42:43], v[50:51]\n" \
  "v_pk_add_f32 v[24:25], v[24:25], v[40:41]\n" \
  "v_pk_add_f32 v[26:27], v[26:27], v[42:43]\n" \
  "s_nop 0\n" \
  "v_add_f32_dpp v56, v24, v24 quad_perm:[1,0,3,2] row_mask:0xf bank_mask:0xf\n" \
  "v_add_f32_dpp v57, v25, v25 quad_perm:[1,0,3,2] row_mask:0xf bank_mask:0xf\n" \
  "v_add_f32_dpp v58, v26, v26 quad_perm:[1,0,3,2] row_mask:0xf bank_mask:0xf\n" \
  "v_add_f32_dpp v59, v27, v27 quad_perm:[1,0,3,2] row_mask:0xf bank_mask:0xf\n" \
  "v_pk_add_f32 v[28:29], v[56:57], v[58:59]\n" \
  "v_add_f32 v28, v28, v29\n" \
  "v_cmp_u_f32 vcc, v28, v28\n" \
  "s_cbranch_vccz LGATH_2_%=\n" \
  "s_cmp_lg_u32 s73, 0\n" \
  "s_cbranch_scc1 LGATH_2_%=\n" \
  "s_sub_u32 s66, s66, 1\n" \
  "s_cmp_lt_i32 s66, 0\n" \
  "s_cbranch_scc1 LGATH_2_%=\n" \
  "s_mov_b32 exec_hi, 0x00ffffff\n" \
  "s_add_u32 s62, s67, 0\n" \
  "s_add_u32 m0, s86, 0\n" \
  "s_nop 0\n" \
  "buffer_load_dwordx4 v61, s[40:43], s62 offen offset:0 sc1 lds\n" \
  "s_add_u32 m0, s86, -2560\n" \
  "s_nop 0\n" \
  "buffer_load_dwordx4 v61, s[40:43], s62 offen offset:3584 sc1 lds\n" \
  "s_add_u32 s62, s67, 7168\n" \
  "s_add_u32 m0, s86, 2048\n" \
  "s_nop 0\n" \
  "buffer_load_dwordx4 v61, s[40:43], s62 offen offset:0 sc1 lds\n" \
  "s_add_u32 m0, s86, -512\n" \
  "s_nop 0\n" \
  "buffer_load_dwordx4 v61, s[40:43], s62 offen offset:3584 sc1 lds\n" \
  "s_add_u32 s62, s67, 14336\n" \
  "s_add_u32 m0, s86, 4096\n" \
  "s_nop 0\n" \
  "buffer_load_dwordx4 v61, s[40:43], s62 offen offset:0 sc1 lds\n" \
  "s_add_u32 m0, s86, 1536\n" \
  "s_nop 0\n" \
  "buffer_load_dwordx4 v61, s[40:43], s62 offen offset:3584 sc1 lds\n" \
  "s_add_u32 s62, s67, 21504\n" \
  "s_add_u32 m0, s86, 6144\n" \
  "s_nop 0\n" \
  "buffer_load_dwordx4 v61, s[40:43], s62 offen offset:0 sc1 lds\n" \
  "s_add_u32 m0, s86, 3584\n" \
  "s_nop 0\n" \
  "buffer_load_dwordx4 v61, s[40:43], s62 offen offset:3584 sc1 lds\n" \
  "s_mov_b32 exec_hi, -1\n" \
  "s_branch LGATH_1_%=\n" \
  "LGATH_3_%=:\n" \
  "s_waitcnt vmcnt(0)\n" \
  "s_branch LGATH_4_%=\n" \
  "LGATH_2_%=:\n" \
  "s_nop 1\n" \
  "s_setpc_b64 s[64:65]\n" \
  "LLNQ_%=:\n" \
  "s_nop 1\n" \
  "v_add_u32 v30, s79, v1\n" \
  "v_mov_b32_dpp v26, v56 row_newbcast:0 row_mask:0xf bank_mask:0xf\n" \
  "v_cvt_pk_bf16_f32 v16, v56, v57\n" \
  "v_cvt_pk_bf16_f32 v17, v58, v59\n" \
  "v_lshlrev_b32 v18, 16, v16\n" \
  "v_and_b32 v19, 0xffff0000, v16\n" \
  "v_lshlrev_b32 v20, 16, v17\n" \
  "v_and_b32 v21, 0xffff0000, v17\n" \
  "v_fma_f32 v18, -v14, v18, v56\n" \
  "v_fma_f32 v19, -v14, v19, v57\n" \
  "v_fma_f32 v20, -v14, v20, v58\n" \
  "v_fma_f32 v21, -v14, v21, v59\n" \
  "v_cvt_pk_bf16_f32 v16, v18, v19\n" \
  "v_cvt_pk_bf16_f32 v17, v20, v21\n" \
  "ds_write_b64 v7, v[16:17]\n" \
  "v_sub_f32 v27, v56, v26\n" \
  "v_sub_f32 v28, v57, v26\n" \
  "v_sub_f32 v29, v58, v26\n" \
  "v_sub_f32 v31, v59, v26\n" \
  "v_add_f32 v24, v27, v28\n" \
  "v_mul_f32 v25, v27, v27\n" \
  "v_add_f32 v22, v29, v31\n" \
  "v_fma_f32 v25, v28, v28, v25\n" \
  "v_add_f32 v24, v24, v22\n" \
  "v_fma_f32 v25, v29, v29, v25\n" \
  "v_cndmask_b32_e64 v24, 0, v24, s[70:71]\n" \
  "v_fma_f32 v25, v31, v31, v25\n" \
  "s_nop 0\n" \
  "v_add_f32_dpp v24, v24, v24 row_ror:8 row_mask:0xf bank_mask:0xf\n" \
  "v_cndmask_b32_e64 v25, 0, v25, s[70:71]\n" \
  "s_mov_b32 exec_lo, 0x15551555\n" \
  "s_mov_b32 exec_hi, 0x15551555\n" \
  "ds_write_b128 v30, v[56:59]\n" \
  "s_mov_b64 exec, -1\n" \
  "v_add_f32_dpp v24, v24, v24 row_ror:4 row_mask:0xf bank_mask:0xf\n" \
  "v_add_f32_dpp v25, v25, v25 row_ror:8 row_mask:0xf bank_mask:0xf\n" \
  "s_nop 0\n" \
  "v_add_f32_dpp v24, v24, v24 row_ror:2 row_mask:0xf bank_mask:0xf\n" \
  "v_add_f32_dpp v25, v25, v25 row_ror:4 row_mask:0xf bank_mask:0xf\n" \
  "s_nop 1\n" \
  "v_add_f32_dpp v25, v25, v25 row_ror:2 row_mask:0xf bank_mask:0xf\n" \
  "s_mov_b32 exec_lo, 0x10001\n" \
  "s_mov_b32 exec_hi, 0x10001\n" \
  "ds_write_b96 v11, v[24:26]\n" \
  "s_mov_b64 exec, -1\n" \
  "s_waitcnt lgkmcnt(0)\n" \
  "s_barrier\n" \
  "ds_read_b96 v[60:62], v13\n" \
  "ds_read_b128 v[16:19], v4 offset:0\n" \
  "ds_read_b128 v[20:23], v4 offset:128\n" \
  "ds_read_b128 v[24:27], v4 offset:256\n" \
  "ds_read_b128 v[28:31], v4 offset:384\n" \
  "ds_read_b128 v[32:35], v4 offset:512\n" \
  "ds_read_b128 v[36:39], v4 offset:640\n" \
  "ds_read_b128 v[40:43], v4 offset:768\n" \
  "v_mov_b64 v[44:45], 0\n" \
  "v_mov_b64 v[46:47], 0\n" \
  "v_mov_b64 v[48:49], 0\n" \
  "v_mov_b64 v[50:51], 0\n" \
  "v_mov_b64 v[52:53], 0\n" \
  "v_mov_b64 v[54:55], 0\n" \
  "v_mov_b64 v[56:57], 0\n" \
  "v_mov_b64 v[58:59], 0\n" \
  "v_xor_b32 v7, 0x800, v7\n" \
  "v_xor_b32 v4, 0x800, v4\n" \
  "v_xor_b32 v11, 0x800, v11\n" \
  "v_xor_b32 v13, 0x800, v13\n" \
  "s_waitcnt lgkmcnt(6)\n" \
  "v_smfmac_f32_16x16x64_bf16 v[44:47], v[16:19], a[0:7], v6\n" \
  "v_fmamk_f32 v62, v60, 0x3d124925, v62\n" \
  "s_waitcnt lgkmcnt(5)\n" \
  "v_smfmac_f32_16x16x64_bf16 v[44:47], v[20:23], a[32:39], v6\n" \
  "v_mul_f32 v63, v60, v60\n" \
  "s_waitcnt lgkmcnt(4)\n" \
  "v_smfmac_f32_16x16x64_bf16 v[44:47], v[24:27], a[64:71], v6\n" \
  "v_add_f32_dpp v60, v62, v62 row_ror:8 row_mask:0xf bank_mask:0xf\n" \
  "s_waitcnt lgkmcnt(3)\n" \
  "v_smfmac_f32_16x16x64_bf16 v[44:47], v[28:31], a[96:103], v6\n" \
  "v_fmamk_f32 v61, v63, 0xbd124925, v61\n" \
  "s_waitcnt lgkmcnt(2)\n" \
  "v_smfmac_f32_16x16x64_bf16 v[44:47], v[32:35], a[128:135], v6\n" \
  "v_add_f32_dpp v60, v60, v60 row_ror:4 row_mask:0xf bank_mask:0xf\n" \
  "s_waitcnt lgkmcnt(1)\n" \
  "v_smfmac_f32_16x16x64_bf16 v[44:47], v[36:39], a[160:167], v6\n" \
  "v_add_f32_dpp v60, v60, v60 row_ror:2 row_mask:0xf bank_mask:0xf\n" \
  "s_waitcnt lgkmcnt(0)\n" \
  "v_smfmac_f32_16x16x64_bf16 v[44:47], v[40:43], a[192:199], v6\n" \
  "v_add_f32_dpp v60, v60, v60 row_ror:1 row_mask:0xf bank_mask:0xf\n" \
  "v_smfmac_f32_16x16x64_bf16 v[48:51], v[16:19], a[8:15], v6\n" \
  "v_mul_f32 v60, 0x3d800000, v60\n" \
  "v_smfmac_f32_16x16x64_bf16 v[48:51], v[20:23], a[40:47], v6\n" \
  "v_sub_f32 v62, v62, v60\n" \
  "v_smfmac_f32_16x16x64_bf16 v[48:51], v[24:27], a[72:79], v6\n" \
  "v_mul_f32 v62, v62, v62\n" \
  "v_smfmac_f32_16x16x64_bf16 v[48:51], v[28:31], a[104:111], v6\n" \
  "v_add_f32 v44, v44, v45\n" \
  "v_smfmac_f32_16x16x64_bf16 v[48:51], v[32:35], a[136:143], v6\n" \
  "v_add_f32 v46, v46, v47\n" \
  "v_smfmac_f32_16x16x64_bf16 v[48:51], v[36:39], a[168:175], v6\n" \
  "v_fmamk_f32 v61, v62, 0x41e00000, v61\n" \
  "v_smfmac_f32_16x16x64_bf16 v[48:51], v[40:43], a[200:207], v6\n" \
  "v_smfmac_f32_16x16x64_bf16 v[52:55], v[16:19], a[16:23], v6\n" \
  "v_add_f32_dpp v61, v61, v61 row_ror:8 row_mask:0xf bank_mask:0xf\n" \
  "v_smfmac_f32_16x16x64_bf16 v[52:55], v[20:23], a[48:55], v6\n" \
  "v_smfmac_f32_16x16x64_bf16 v[52:55], v[24:27], a[80:87], v6\n" \
  "v_add_f32_dpp v61, v61, v61 row_ror:4 row_mask:0xf bank_mask:0xf\n" \
  "v_smfmac_f32_16x16x64_bf16 v[52:55], v[28:31], a[112:119], v6\n" \
  "v_smfmac_f32_16x16x64_bf16 v[52:55], v[32:35], a[144:151], v6\n" \
  "v_add_f32_dpp v61, v61, v61 row_ror:2 row_mask:0xf bank_mask:0xf\n" \
  "v_smfmac_f32_16x16x64_bf16 v[52:55], v[36:39], a[176:183], v6\n" \
  "v_add_f32 v48, v48, v49\n" \
  "v_smfmac_f32_16x16x64_bf16 v[52:55], v[40:43], a[208:215], v6\n" \
  "v_add_f32 v50, v50, v51\n" \
  "v_smfmac_f32_16x16x64_bf16 v[56:59], v[16:19], a[24:31], v6\n" \
  "v_add_f32_dpp v61, v61, v61 row_ror:1 row_mask:0xf bank_mask:0xf\n" \
  "v_smfmac_f32_16x16x64_bf16 v[56:59], v[20:23], a[56:63], v6\n" \
  "v_add_f32 v61, 0x3b92ccf7, v61\n" \
  "v_smfmac_f32_16x16x64_bf16 v[56:59], v[24:27], a[88:95], v6\n" \
  "v_rsq_f32 v61, v61\n" \
  "v_smfmac_f32_16x16x64_bf16 v[56:59], v[28:31], a[120:127], v6\n" \
  "v_add_f32 v52, v52, v53\n" \
  "v_smfmac_f32_16x16x64_bf16 v[56:59], v[32:35], a[152:159], v6\n" \
  "v_add_f32 v54, v54, v55\n" \
  "v_smfmac_f32_16x16x64_bf16 v[56:59], v[36:39], a[184:191], v6\n" \
  "v_smfmac_f32_16x16x64_bf16 v[56:59], v[40:43], a[216:223], v6\n" \
  "s_nop 7\n" \
  "v_add_f32 v56, v56, v57\n" \
  "v_add_f32 v58, v58, v59\n" \
  "s_nop 1\n" \
  "s_setpc_b64 s[64:65]\n" \
  "LQOUT_%=:\n" \
  "s_nop 1\n" \
  "v_add_f32_dpp v36, v44, v46 quad_perm:[0,1,2,3] row_mask:0x1 bank_mask:0xf\n" \
  "v_add_f32_dpp v36, v48, v50 quad_perm:[0,1,2,3] row_mask:0x2 bank_mask:0xf\n" \
  "v_add_f32_dpp v36, v52, v54 quad_perm:[0,1,2,3] row_mask:0x4 bank_mask:0xf\n" \
  "v_add_f32_dpp v36, v56, v58 quad_perm:[0,1,2,3] row_mask:0x8 bank_mask:0xf\n" \
  "v_mul_f32 v54, 0x3ff449ee, v61\n" \
  "s_cmp_lg_u32 s73, 0\n" \
  "s_cbranch_scc1 LQOUT_1_%=\n" \
  "v_fma_f32 v36, -v60, v10, v36\n" \
  "v_mul_f32 v36, v54, v36\n" \
  "v_cvt_pk_bf16_f32 v38, v36, v36\n" \
  "buffer_store_short v38, v8, s[40:43], s67 offen sc1\n" \
  "s_branch LQOUT_2_%=\n" \
  "LQOUT_1_%=:\n" \
  "v_mov_b32 v10, v36\n" \
  "LQOUT_2_%=:\n" \
  "s_nop 1\n" \
  "s_setpc_b64 s[64:65]\n" \
  "LHB_%=:\n" \
  "s_nop 1\n" \
  "v_add_f32_dpp v36, v44, v46 quad_perm:[0,1,2,3] row_mask:0x5 bank_mask:0xf\n" \
  "v_add_f32_dpp v36, v48, v50 quad_perm:[0,1,2,3] row_mask:0xa bank_mask:0xf\n" \
  "v_add_f32_dpp v37, v52, v54 quad_perm:[0,1,2,3] row_mask:0x5 bank_mask:0xf\n" \
  "v_add_f32_dpp v37, v56, v58 quad_perm:[0,1,2,3] row_mask:0xa bank_mask:0xf\n" \
  "v_mul_f32 v40, v61, v61\n" \
  "s_cmp_eq_u32 s73, 0\n" \
  "s_cbranch_scc1 LHB_1_%=\n" \
  "v_mov_b32 v2, v36\n" \
  "v_mov_b32 v3, v37\n" \
  "LHB_1_%=:\n" \
  "v_fma_f32 v36, -v60, v2, v36\n" \
  "v_fma_f32 v37, -v60, v3, v37\n" \
  "v_mul_f32 v40, 0x43e00000, v40\n" \
  "v_mul_f32 v36, v36, v37\n" \
  "v_mul_f32 v36, v36, v40\n" \
  "v_cvt_pk_bf16_f32 v38, v36, v36\n" \
  "v_lshlrev_b32 v39, 16, v38\n" \
  "v_fma_f32 v36, -v15, v39, v36\n" \
  "v_cvt_pk_bf16_f32 v38, v36, v36\n" \
  "ds_write_b16 v9, v38\n" \
  "s_nop 0\n" \
  "s_setpc_b64 s[64:65]\n" \
  "LGO_%=:\n" \
  "s_nop 1\n" \
  "v_add_u32 v62, s79, v12\n" \
  "s_waitcnt lgkmcnt(0)\n" \
  "s_barrier\n" \
  "ds_read_b128 v[16:19], v5 offset:0\n" \
  "ds_read_b128 v[20:23], v5 offset:128\n" \
  "s_cmp_eq_u32 s68, 0\n" \
  "s_cbranch_scc1 LGO_1_%=\n" \
  "ds_read_b128 v[24:27], v5 offset:256\n" \
  "ds_read_b128 v[28:31], v5 offset:384\n" \
  "ds_read_b32 v63, v62 offset:256\n" \
  "ds_read_b32 v62, v62\n" \
  "v_mov_b64 v[32:33], 0\n" \
  "v_mov_b64 v[34:35], 0\n" \
  "v_mov_b64 v[36:37], 0\n" \
  "v_mov_b64 v[38:39], 0\n" \
  "v_mov_b64 v[40:41], 0\n" \
  "v_mov_b64 v[42:43], 0\n" \
  "v_mov_b64 v[44:45], 0\n" \
  "v_mov_b64 v[46:47], 0\n" \
  "v_mov_b64 v[48:49], 0\n" \
  "v_mov_b64 v[50:51], 0\n" \
  "v_mov_b64 v[52:53], 0\n" \
  "v_mov_b64 v[54:55], 0\n" \
  "v_mov_b64 v[56:57], 0\n" \
  "v_mov_b64 v[58:59], 0\n" \
  "v_xor_b32 v5, 0x400, v5\n" \
  "v_xor_b32 v9, 0x400, v9\n" \
  "s_waitcnt lgkmcnt(5)\n" \
  "v_smfmac_f32_16x16x64_bf16 v[32:35], v[16:19], a[224:231], v6\n" \
  "s_waitcnt lgkmcnt(4)\n" \
  "v_smfmac_f32_16x16x64_bf16 v[32:35], v[20:23], v[88:95], v6\n" \
  "s_waitcnt lgkmcnt(3)\n" \
  "v_smfmac_f32_16x16x64_bf16 v[32:35], v[24:27], v[144:151], v6\n" \
  "s_waitcnt lgkmcnt(2)\n" \
  "v_smfmac_f32_16x16x64_bf16 v[32:35], v[28:31], v[200:207], v6\n" \
  "v_smfmac_f32_16x16x64_bf16 v[36:39], v[16:19], a[232:239], v6\n" \
  "v_smfmac_f32_16x16x64_bf16 v[36:39], v[20:23], v[96:103], v6\n" \
  "v_smfmac_f32_16x16x64_bf16 v[36:39], v[24:27], v[152:159], v6\n" \
  "v_smfmac_f32_16x16x64_bf16 v[36:39], v[28:31], v[208:215], v6\n" \
  "v_smfmac_f32_16x16x64_bf16 v[40:43], v[16:19], a[240:247], v6\n" \
  "v_smfmac_f32_16x16x64_bf16 v[40:43], v[20:23], v[104:111], v6\n" \
  "v_smfmac_f32_16x16x64_bf16 v[40:43], v[24:27], v[160:167], v6\n" \
  "v_smfmac_f32_16x16x64_bf16 v[40:43], v[28:31], v[216:223], v6\n" \
  "v_add_f32 v32, v32, v33\n" \
  "v_smfmac_f32_16x16x64_bf16 v[44:47], v[16:19], a[248:255], v6\n" \
  "v_add_f32 v34, v34, v35\n" \
  "v_smfmac_f32_16x16x64_bf16 v[44:47], v[20:23], v[112:119], v6\n" \
  "v_add_f32_dpp v60, v32, v34 quad_perm:[0,1,2,3] row_mask:0x1 bank_mask:0xf\n" \
  "v_smfmac_f32_16x16x64_bf16 v[44:47], v[24:27], v[168:175], v6\n" \
  "v_add_f32 v36, v36, v37\n" \
  "v_smfmac_f32_16x16x64_bf16 v[44:47], v[28:31], v[224:231], v6\n" \
  "v_add_f32 v38, v38, v39\n" \
  "v_smfmac_f32_16x16x64_bf16 v[48:51], v[16:19], v[64:71], v6\n" \
  "v_add_f32_dpp v60, v36, v38 quad_perm:[0,1,2,3] row_mask:0x2 bank_mask:0xf\n" \
  "v_smfmac_f32_16x16x64_bf16 v[48:51], v[20:23], v[120:127], v6\n" \
  "v_add_f32 v40, v40, v41\n" \
  "v_smfmac_f32_16x16x64_bf16 v[48:51], v[24:27], v[176:183], v6\n" \
  "v_add_f32 v42, v42, v43\n" \
  "v_smfmac_f32_16x16x64_bf16 v[48:51], v[28:31], v[232:239], v6\n" \
  "v_add_f32_dpp v60, v40, v42 quad_perm:[0,1,2,3] row_mask:0x4 bank_mask:0xf\n" \
  "v_smfmac_f32_16x16x64_bf16 v[52:55], v[16:19], v[72:79], v6\n" \
  "v_add_f32 v44, v44, v45\n" \
  "v_smfmac_f32_16x16x64_bf16 v[52:55], v[20:23], v[128:135], v6\n" \
  "v_add_f32 v46, v46, v47\n" \
  "v_smfmac_f32_16x16x64_bf16 v[52:55], v[24:27], v[184:191], v6\n" \
  "v_add_f32_dpp v60, v44, v46 quad_perm:[0,1,2,3] row_mask:0x8 bank_mask:0xf\n" \
  "v_smfmac_f32_16x16x64_bf16 v[52:55], v[28:31], v[240:247], v6\n" \
  "v_add_f32 v48, v48, v49\n" \
  "v_smfmac_f32_16x16x64_bf16 v[56:59], v[16:19], v[80:87], v6\n" \
  "v_add_f32 v50, v50, v51\n" \
  "v_smfmac_f32_16x16x64_bf16 v[56:59], v[20:23], v[136:143], v6\n" \
  "v_add_f32_dpp v61, v48, v50 quad_perm:[0,1,2,3] row_mask:0x1 bank_mask:0xf\n" \
  "v_smfmac_f32_16x16x64_bf16 v[56:59], v[24:27], v[192:199], v6\n" \
  "v_smfmac_f32_16x16x64_bf16 v[56:59], v[28:31], v[248:255], v6\n" \
  "s_nop 0\n" \
  "v_add_f32 v52, v52, v53\n" \
  "v_add_f32 v54, v54, v55\n" \
  "s_nop 0\n" \
  "v_add_f32_dpp v61, v52, v54 quad_perm:[0,1,2,3] row_mask:0x2 bank_mask:0xf\n" \
  "s_nop 2\n" \
  "v_add_f32 v56, v56, v57\n" \
  "v_add_f32 v58, v58, v59\n" \
  "s_nop 0\n" \
  "v_add_f32_dpp v61, v56, v58 quad_perm:[0,1,2,3] row_mask:0x4 bank_mask:0xf\n" \
  "s_waitcnt lgkmcnt(0)\n" \
  "s_branch LGO_2_%=\n" \
  "LGO_1_%=:\n" \
  "ds_read_b32 v63, v62 offset:256\n" \
  "ds_read_b32 v62, v62\n" \
  "v_mov_b64 v[32:33], 0\n" \
  "v_mov_b64 v[34:35], 0\n" \
  "v_mov_b64 v[36:37], 0\n" \
  "v_mov_b64 v[38:39], 0\n" \
  "v_mov_b64 v[40:41], 0\n" \
  "v_mov_b64 v[42:43], 0\n" \
  "v_mov_b64 v[44:45], 0\n" \
  "v_mov_b64 v[46:47], 0\n" \
  "v_mov_b64 v[48:49], 0\n" \
  "v_mov_b64 v[50:51], 0\n" \
  "v_mov_b64 v[52:53], 0\n" \
  "v_mov_b64 v[54:55], 0\n" \
  "v_mov_b64 v[56:57], 0\n" \
  "v_mov_b64 v[58:59], 0\n" \
  "v_xor_b32 v5, 0x400, v5\n" \
  "v_xor_b32 v9, 0x400, v9\n" \
  "s_waitcnt lgkmcnt(3)\n" \
  "v_smfmac_f32_16x16x64_bf16 v[32:35], v[16:19], a[224:231], v6\n" \
  "s_waitcnt lgkmcnt(2)\n" \
  "v_smfmac_f32_16x16x64_bf16 v[32:35], v[20:23], v[88:95], v6\n" \
  "v_smfmac_f32_16x16x64_bf16 v[36:39], v[16:19], a[232:239], v6\n" \
  "v_smfmac_f32_16x16x64_bf16 v[36:39], v[20:23], v[96:103], v6\n" \
  "v_smfmac_f32_16x16x64_bf16 v[40:43], v[16:19], a[240:247], v6\n" \
  "v_smfmac_f32_16x16x64_bf16 v[40:43], v[20:23], v[104:111], v6\n" \
  "v_smfmac_f32_16x16x64_bf16 v[44:47], v[16:19], a[248:255], v6\n" \
  "v_smfmac_f32_16x16x64_bf16 v[44:47], v[20:23], v[112:119], v6\n" \
  "v_smfmac_f32_16x16x64_bf16 v[48:51], v[16:19], v[64:71], v6\n" \
  "v_smfmac_f32_16x16x64_bf16 v[48:51], v[20:23], v[120:127], v6\n" \
  "v_add_f32 v32, v32, v33\n" \
  "v_add_f32 v34, v34, v35\n" \
  "v_smfmac_f32_16x16x64_bf16 v[52:55], v[16:19], v[72:79], v6\n" \
  "v_add_f32_dpp v60, v32, v34 quad_perm:[0,1,2,3] row_mask:0x1 bank_mask:0xf\n" \
  "v_add_f32 v36, v36, v37\n" \
  "v_smfmac_f32_16x16x64_bf16 v[52:55], v[20:23], v[128:135], v6\n" \
  "v_add_f32 v38, v38, v39\n" \
  "v_add_f32_dpp v60, v36, v38 quad_perm:[0,1,2,3] row_mask:0x2 bank_mask:0xf\n" \
  "v_smfmac_f32_16x16x64_bf16 v[56:59], v[16:19], v[80:87], v6\n" \
  "v_add_f32 v40, v40, v41\n" \
  "v_add_f32 v42, v42, v43\n" \
  "v_smfmac_f32_16x16x64_bf16 v[56:59], v[20:23], v[136:143], v6\n" \
  "v_add_f32_dpp v60, v40, v42 quad_perm:[0,1,2,3] row_mask:0x4 bank_mask:0xf\n" \
  "v_add_f32 v44, v44, v45\n" \
  "v_add_f32 v46, v46, v47\n" \
  "s_nop 0\n" \
  "v_add_f32_dpp v60, v44, v46 quad_perm:[0,1,2,3] row_mask:0x8 bank_mask:0xf\n" \
  "v_add_f32 v48, v48, v49\n" \
  "v_add_f32 v50, v50, v51\n" \
  "s_nop 0\n" \
  "v_add_f32_dpp v61, v48, v50 quad_perm:[0,1,2,3] row_mask:0x1 bank_mask:0xf\n" \
  "v_add_f32 v52, v52, v53\n" \
  "v_add_f32 v54, v54, v55\n" \
  "s_nop 0\n" \
  "v_add_f32_dpp v61, v52, v54 quad_perm:[0,1,2,3] row_mask:0x2 bank_mask:0xf\n" \
  "v_add_f32 v56, v56, v57\n" \
  "v_add_f32 v58, v58, v59\n" \
  "s_nop 0\n" \
  "v_add_f32_dpp v61, v56, v58 quad_perm:[0,1,2,3] row_mask:0x4 bank_mask:0xf\n" \
  "s_waitcnt lgkmcnt(0)\n" \
  "LGO_2_%=:\n" \
  "s_nop 1\n" \
  "v_fmac_f32 v60, s78, v62\n" \
  "v_fmac_f32 v61, s78, v63\n" \
  "s_cmp_lg_u32 s73, 0\n" \
  "s_cbranch_scc1 LGO_3_%=\n" \
  "buffer_store_dword v60, v12, s[40:43], s67 offen sc1\n" \
  "s_mov_b32 exec_hi, 0x0000ffff\n" \
  "buffer_store_dword v61, v12, s[40:43], s67 offen offset:256 sc1\n" \
  "s_mov_b64 exec, -1\n" \
  "LGO_3_%=:\n" \
  "s_setpc_b64 s[64:65]\n" \
  "LPG_%=:\n" \
  "s_nop 1\n" \
  "v_lshlrev_b32 v63, 4, v0\n" \
  "v_lshrrev_b32 v60, 4, v0\n" \
  "v_lshlrev_b32 v60, 5, v60\n" \
  "v_and_b32 v61, 1, v0\n" \
  "v_lshl_add_u32 v60, v61, 4, v60\n" \
  "v_sub_u32 v61, v3, v60\n" \
  "v_sub_u32 v60, v2, v60\n" \
  "s_mov_b32 m0, s87\n" \
  "s_nop 0\n" \
  "buffer_load_dwordx4 v63, s[40:43], s67 offen offset:0 sc1 lds\n" \
  "buffer_load_dwordx4 v63, s[40:43], s67 offen offset:1024 sc1 lds\n" \
  "buffer_load_dwordx4 v63, s[40:43], s67 offen offset:2048 sc1 lds\n" \
  "buffer_load_dwordx4 v63, s[40:43], s67 offen offset:3072 sc1 lds\n" \
  "s_cmp_lg_u32 s73, 0\n" \
  "s_cbranch_scc1 LPG_3_%=\n" \
  "s_sleep 12\n" \
  "buffer_load_dwordx4 v63, s[40:43], s67 offen offset:0 sc1 lds\n" \
  "buffer_load_dwordx4 v63, s[40:43], s67 offen offset:1024 sc1 lds\n" \
  "buffer_load_dwordx4 v63, s[40:43], s67 offen offset:2048 sc1 lds\n" \
  "buffer_load_dwordx4 v63, s[40:43], s67 offen offset:3072 sc1 lds\n" \
  "LPG_1_%=:\n" \
  "s_waitcnt vmcnt(4)\n" \
  "LPG_4_%=:\n" \
  "s_nop 1\n" \
  "ds_read_b128 v[40:43], v60 offset:256\n" \
  "ds_read_b128 v[52:55], v61 offset:256\n" \
  "ds_read_b128 v[32:35], v2 offset:0\n" \
  "ds_read_b128 v[44:47], v3 offset:0\n" \
  "ds_read_b128 v[36:39], v2 offset:128\n" \
  "ds_read_b128 v[48:51], v3 offset:128\n" \
  "s_waitcnt lgkmcnt(4)\n" \
  "v_pk_add_f32 v[58:59], v[40:41], v[52:53]\n" \
  "v_max_f32 v56, v40, v52\n" \
  "s_nop 1\n" \
  "v_max_f32_dpp v56, v56, v56 row_ror:2 row_mask:0xf bank_mask:0xf\n" \
  "s_nop 1\n" \
  "v_max_f32_dpp v56, v56, v56 row_ror:4 row_mask:0xf bank_mask:0xf\n" \
  "s_nop 1\n" \
  "v_max_f32_dpp v56, v56, v56 row_ror:8 row_mask:0xf bank_mask:0xf\n" \
  "v_sub_f32 v57, v40, v56\n" \
  "v_sub_f32 v56, v52, v56\n" \
  "v_exp_f32 v57, v57\n" \
  "v_exp_f32 v56, v56\n" \
  "s_waitcnt lgkmcnt(2)\n" \
  "v_pk_add_f32 v[24:25], v[32:33], v[44:45]\n" \
  "v_pk_add_f32 v[26:27], v[34:35], v[46:47]\n" \
  "s_waitcnt lgkmcnt(0)\n" \
  "v_pk_add_f32 v[28:29], v[36:37], v[48:49]\n" \
  "v_pk_add_f32 v[30:31], v[38:39], v[50:51]\n" \
  "v_pk_add_f32 v[24:25], v[24:25], v[28:29]\n" \
  "v_pk_add_f32 v[26:27], v[26:27], v[30:31]\n" \
  "v_pk_add_f32 v[24:25], v[24:25], v[26:27]\n" \
  "v_pk_add_f32 v[24:25], v[24:25], v[58:59]\n" \
  "v_add_f32 v24, v24, v25\n" \
  "v_cmp_u_f32 vcc, v24, v24\n" \
  "s_cbranch_vccz LPG_2_%=\n" \
  "s_cmp_lg_u32 s73, 0\n" \
  "s_cbranch_scc1 LPG_2_%=\n" \
  "s_sub_u32 s66, s66, 1\n" \
  "s_cmp_lt_i32 s66, 0\n" \
  "s_cbranch_scc1 LPG_2_%=\n" \
  "buffer_load_dwordx4 v63, s[40:43], s67 offen offset:0 sc1 lds\n" \
  "buffer_load_dwordx4 v63, s[40:43], s67 offen offset:1024 sc1 lds\n" \
  "buffer_load_dwordx4 v63, s[40:43], s67 offen offset:2048 sc1 lds\n" \
  "buffer_load_dwordx4 v63, s[40:43], s67 offen offset:3072 sc1 lds\n" \
  "s_branch LPG_1_%=\n" \
  "LPG_3_%=:\n" \
  "s_waitcnt vmcnt(0)\n" \
  "s_branch LPG_4_%=\n" \
  "LPG_2_%=:\n" \
  "s_nop 1\n" \
  "s_mov_b32 s74, 0x0fff0fff\n" \
  "s_mov_b32 s75, 0x0fff0fff\n" \
  "v_cndmask_b32_e64 v56, 0, v56, s[74:75]\n" \
  "v_mul_f32 v32, v57, v32\n" \
  "v_mul_f32 v33, v57, v33\n" \
  "v_mul_f32 v34, v57, v34\n" \
  "v_mul_f32 v35, v57, v35\n" \
  "v_mul_f32 v36, v57, v36\n" \
  "v_mul_f32 v37, v57, v37\n" \
  "v_mul_f32 v38, v57, v38\n" \
  "v_mul_f32 v39, v57, v39\n" \
  "v_mul_f32 v59, v57, v41\n" \
  "v_fmac_f32 v32, v56, v44\n" \
  "v_fmac_f32 v36, v56, v48\n" \
  "v_fmac_f32 v33, v56, v45\n" \
  "v_fmac_f32 v37, v56, v49\n" \
  "v_fmac_f32 v34, v56, v46\n" \
  "v_fmac_f32 v38, v56, v50\n" \
  "v_fmac_f32 v35, v56, v47\n" \
  "v_fmac_f32 v39, v56, v51\n" \
  "v_fmac_f32 v59, v56, v53\n" \
  "v_cndmask_b32_e64 v44, v36, v32, s[20:21]\n" \
  "v_cndmask_b32_e64 v45, v37, v33, s[20:21]\n" \
  "v_cndmask_b32_e64 v46, v38, v34, s[20:21]\n" \
  "v_cndmask_b32_e64 v47, v39, v35, s[20:21]\n" \
  "v_add_f32_dpp v59, v59, v59 quad_perm:[2,3,0,1] row_mask:0xf bank_mask:0xf\n" \
  "v_cndmask_b32_e64 v32, v32, v36, s[20:21]\n" \
  "v_cndmask_b32_e64 v33, v33, v37, s[20:21]\n" \
  "v_cndmask_b32_e64 v34, v34, v38, s[20:21]\n" \
  "v_cndmask_b32_e64 v35, v35, v39, s[20:21]\n" \
  "v_add_f32_dpp v59, v59, v59 row_ror:4 row_mask:0xf bank_mask:0xf\n" \
  "v_add_f32_dpp v32, v44, v32 quad_perm:[2,3,0,1] row_mask:0xf bank_mask:0xf\n" \
  "v_add_f32_dpp v33, v45, v33 quad_perm:[2,3,0,1] row_mask:0xf bank_mask:0xf\n" \
  "v_add_f32_dpp v34, v46, v34 quad_perm:[2,3,0,1] row_mask:0xf bank_mask:0xf\n" \
  "v_add_f32_dpp v35, v47, v35 quad_perm:[2,3,0,1] row_mask:0xf bank_mask:0xf\n" \
  "v_add_f32_dpp v59, v59, v59 row_ror:8 row_mask:0xf bank_mask:0xf\n" \
  "v_add_f32_dpp v32, v32, v32 row_ror:4 row_mask:0xf bank_mask:0xf\n" \
  "v_add_f32_dpp v33, v33, v33 row_ror:4 row_mask:0xf bank_mask:0xf\n" \
  "v_add_f32_dpp v34, v34, v34 row_ror:4 row_mask:0xf bank_mask:0xf\n" \
  "v_add_f32_dpp v35, v35, v35 row_ror:4 row_mask:0xf bank_mask:0xf\n" \
  "v_rcp_f32 v59, v59\n" \
  "v_add_f32_dpp v32, v32, v32 row_ror:8 row_mask:0xf bank_mask:0xf\n" \
  "v_add_f32_dpp v33, v33, v33 row_ror:8 row_mask:0xf bank_mask:0xf\n" \
  "v_add_f32_dpp v34, v34, v34 row_ror:8 row_mask:0xf bank_mask:0xf\n" \
  "v_add_f32_dpp v35, v35, v35 row_ror:8 row_mask:0xf bank_mask:0xf\n" \
  "v_mul_f32 v32, v59, v32\n" \
  "v_mul_f32 v33, v59, v33\n" \
  "v_mul_f32 v34, v59, v34\n" \
  "v_mul_f32 v35, v59, v35\n" \
  "v_cvt_pk_bf16_f32 v16, v32, v33\n" \
  "v_cvt_pk_bf16_f32 v17, v34, v35\n" \
  "v_lshlrev_b32 v18, 16, v16\n" \
  "v_and_b32 v19, 0xffff0000, v16\n" \
  "v_lshlrev_b32 v20, 16, v17\n" \
  "v_and_b32 v21, 0xffff0000, v17\n" \
  "v_fma_f32 v18, -v15, v18, v32\n" \
  "v_fma_f32 v19, -v15, v19, v33\n" \
  "v_fma_f32 v20, -v15, v20, v34\n" \
  "v_fma_f32 v21, -v15, v21, v35\n" \
  "v_cvt_pk_bf16_f32 v16, v18, v19\n" \
  "v_cvt_pk_bf16_f32 v17, v20, v21\n" \
  "ds_write_b64 v9, v[16:17]\n" \
  "s_nop 0\n" \
  "s_setpc_b64 s[64:65]\n" \
  "LFIN_%=:\n" \
  "s_nop 1\n" \
  "s_cmp_gt_u32 s60, 1\n" \
  "s_cbranch_scc1 LFIN_1_%=\n" \
  "v_lshrrev_b32 v60, 3, v0\n" \
  "v_min_u32 v60, 6, v60\n" \
  "s_movk_i32 s63, 0xe00\n" \
  "v_mul_u32_u24 v60, s63, v60\n" \
  "v_and_b32 v61, 7, v0\n" \
  "v_lshl_add_u32 v60, v61, 4, v60\n" \
  "v_lshlrev_b32 v61, 3, v61\n" \
  "s_mov_b32 s74, -1\n" \
  "s_mov_b32 s75, 0x00ffffff\n" \
  "buffer_load_dwordx4 v[32:35], v60, s[40:43], s67 offen sc1\n" \
  "buffer_load_dwordx4 v[36:39], v60, s[40:43], s67 offen offset:1792 sc1\n" \
  "s_cmp_lg_u32 s73, 0\n" \
  "s_cbranch_scc1 LFIN_4_%=\n" \
  "s_sleep 5\n" \
  "buffer_load_dwordx4 v[32:35], v60, s[40:43], s67 offen sc1\n" \
  "buffer_load_dwordx4 v[36:39], v60, s[40:43], s67 offen offset:1792 sc1\n" \
  "s_sleep 5\n" \
  "buffer_load_dwordx4 v[32:35], v60, s[40:43], s67 offen sc1\n" \
  "buffer_load_dwordx4 v[36:39], v60, s[40:43], s67 offen offset:1792 sc1\n" \
  "s_sleep 5\n" \
  "buffer_load_dwordx4 v[32:35], v60, s[40:43], s67 offen sc1\n" \
  "buffer_load_dwordx4 v[36:39], v60, s[40:43], s67 offen offset:1792 sc1\n" \
  "LFIN_2_%=:\n" \
  "s_waitcnt vmcnt(6)\n" \
  "LFIN_5_%=:\n" \
  "s_nop 1\n" \
  "v_pk_add_f32 v[40:41], v[32:33], v[36:37]\n" \
  "v_pk_add_f32 v[42:43], v[34:35], v[38:39]\n" \
  "v_cndmask_b32_e64 v40, 0, v40, s[74:75]\n" \
  "v_cndmask_b32_e64 v41, 0, v41, s[74:75]\n" \
  "v_cndmask_b32_e64 v42, 0, v42, s[74:75]\n" \
  "v_cndmask_b32_e64 v43, 0, v43, s[74:75]\n" \
  "v_add_f32_dpp v40, v40, v40 row_ror:8 row_mask:0xf bank_mask:0xf\n" \
  "v_add_f32_dpp v41, v41, v41 row_ror:8 row_mask:0xf bank_mask:0xf\n" \
  "v_add_f32_dpp v42, v42, v42 row_ror:8 row_mask:0xf bank_mask:0xf\n" \
  "v_add_f32_dpp v43, v43, v43 row_ror:8 row_mask:0xf bank_mask:0xf\n" \
  "v_mov_b32 v44, v40\n" \
  "v_mov_b32 v45, v41\n" \
  "v_mov_b32 v46, v42\n" \
  "v_mov_b32 v47, v43\n" \
  "v_permlane16_swap_b32 v40, v44\n" \
  "v_permlane16_swap_b32 v41, v45\n" \
  "v_permlane16_swap_b32 v42, v46\n" \
  "v_permlane16_swap_b32 v43, v47\n" \
  "v_pk_add_f32 v[40:41], v[40:41], v[44:45]\n" \
  "v_pk_add_f32 v[42:43], v[42:43], v[46:47]\n" \
  "v_mov_b32 v44, v40\n" \
  "v_mov_b32 v45, v41\n" \
  "v_mov_b32 v46, v42\n" \
  "v_mov_b32 v47, v43\n" \
  "v_permlane32_swap_b32 v40, v44\n" \
  "v_permlane32_swap_b32 v41, v45\n" \
  "v_permlane32_swap_b32 v42, v46\n" \
  "v_permlane32_swap_b32 v43, v47\n" \
  "v_pk_add_f32 v[40:41], v[40:41], v[44:45]\n" \
  "v_pk_add_f32 v[42:43], v[42:43], v[46:47]\n" \
  "v_pk_add_f32 v[44:45], v[40:41], v[42:43]\n" \
  "v_add_f32 v44, v44, v45\n" \
  "v_cmp_u_f32 vcc, v44, v44\n" \
  "s_cbranch_vccz LFIN_3_%=\n" \
  "s_cmp_lg_u32 s73, 0\n" \
  "s_cbranch_scc1 LFIN_3_%=\n" \
  "s_sub_u32 s66, s66, 1\n" \
  "s_cmp_lt_i32 s66, 0\n" \
  "s_cbranch_scc1 LFIN_3_%=\n" \
  "buffer_load_dwordx4 v[32:35], v60, s[40:43], s67 offen sc1\n" \
  "buffer_load_dwordx4 v[36:39], v60, s[40:43], s67 offen offset:1792 sc1\n" \
  "s_branch LFIN_2_%=\n" \
  "LFIN_4_%=:\n" \
  "s_waitcnt vmcnt(0)\n" \
  "s_branch LFIN_5_%=\n" \
  "LFIN_3_%=:\n" \
  "s_nop 1\n" \
  "v_cvt_pk_bf16_f32 v44, v40, v41\n" \
  "v_cvt_pk_bf16_f32 v45, v42, v43\n" \
  "s_cmp_lg_u32 s73, 0\n" \
  "s_cbranch_scc1 LFIN_1_%=\n" \
  "s_mov_b32 exec_lo, 0xff\n" \
  "s_mov_b32 exec_hi, 0x0\n" \
  "buffer_store_dwordx2 v[44:45], v61, s[56:59], s68 offen\n" \
  "s_mov_b64 exec, -1\n" \
  "LFIN_1_%=:\n" \
  "s_setpc_b64 s[64:65]\n" \
  "LRDY_%=:\n" \
  "s_nop 1\n" \
  "v_lshlrev_b32 v32, 2, v0\n" \
  "s_mov_b32 s63, 1255424\n" \
  "LRDY_1_%=:\n" \
  "buffer_load_dword v33, v32, s[40:43], s63 offen sc1\n" \
  "s_waitcnt vmcnt(0)\n" \
  "v_cmp_ne_u32 vcc, s80, v33\n" \
  "s_nop 1\n" \
  "s_and_b32 vcc_lo, vcc_lo, 0xffff\n" \
  "s_cmp_eq_u32 vcc_lo, 0\n" \
  "s_cbranch_scc1 LRDY_2_%=\n" \
  "s_sub_u32 s66, s66, 1\n" \
  "s_cmp_lt_i32 s66, 0\n" \
  "s_cbranch_scc1 LRDY_2_%=\n" \
  "s_sleep 4\n" \
  "s_branch LRDY_1_%=\n" \
  "LRDY_2_%=:\n" \
  "s_nop 1\n" \
  "s_setpc_b64 s[64:65]\n" \
  "LCAN_%=:\n" \
  "s_nop 1\n" \
  "s_cmp_lg_u32 s73, 0\n" \
  "s_cbranch_scc1 LCAN_2_%=\n" \
  "s_cmp_lg_u32 s60, 0\n" \
  "s_cbranch_scc1 LCAN_2_%=\n" \
  "v_mul_u32_u24 v60, s68, v0\n" \
  "s_mov_b32 exec_lo, s69\n" \
  "s_mov_b32 exec_hi, 0\n" \
  "LCAN_1_%=:\n" \
  "buffer_load_dword v61, v60, s[40:43], s67 offen sc1\n" \
  "s_waitcnt vmcnt(0)\n" \
  "v_cmp_ne_u32 vcc, -1, v61\n" \
  "s_nop 1\n" \
  "s_bcnt1_i32_b32 s63, vcc_lo\n" \
  "s_cmp_ge_u32 s63, s90\n" \
  "s_cbranch_scc1 LCAN_2_%=\n" \
  "s_sub_u32 s66, s66, 1\n" \
  "s_cmp_lt_i32 s66, 0\n" \
  "s_cbranch_scc1 LCAN_2_%=\n" \
  "s_sleep 1\n" \
  "s_branch LCAN_1_%=\n" \
  "LCAN_2_%=:\n" \
  "s_mov_b64 exec, -1\n" \
  "s_barrier\n" \
  "s_nop 1\n" \
  "s_setpc_b64 s[64:65]\n" \
  "Lsend_%=:\n" \
  ""
#define SVC_CLOB "v0", "v1", "v2", "v3", "v4", "v5", "v6", "v7", "v8", "v9", "v10", "v11", "v12", "v13", "v14", "v15", "v16", "v17", "v18", "v19", "v20", "v21", "v22", "v23", "v24", "v25", "v26", "v27", "v28", "v29", "v30", "v31", "v32", "v33", "v34", "v35", "v36", "v37", "v38", "v39", "v40", "v41", "v42", "v43", "v44", "v45", "v46", "v47", "v48", "v49", "v50", "v51", "v52", "v53", "v54", "v55", "v56", "v57", "v58", "v59", "v60", "v61", "v62", "v63", "v64", "v65", "v66", "v67", "v68", "v69", "v70", "v71", "v72", "v73", "v74", "v75", "v76", "v77", "v78", "v79", "v80", "v81", "v82", "v83", "v84", "v85", "v86", "v87", "v88", "v89", "v90", "v91", "v92", "v93", "v94", "v95", "v96", "v97", "v98", "v99", "v100", "v101", "v102", "v103", "v104", "v105", "v106", "v107", "v108", "v109", "v110", "v111", "v112", "v113", "v114", "v115", "v116", "v117", "v118", "v119", "v120", "v121", "v122", "v123", "v124", "v125", "v126", "v127", "v128", "v129", "v130", "v131", "v132", "v133", "v134", "v135", "v136", "v137", "v138", "v139", "v140", "v141", "v142", "v143", "v144", "v145", "v146", "v147", "v148", "v149", "v150", "v151", "v152", "v153", "v154", "v155", "v156", "v157", "v158", "v159", "v160", "v161", "v162", "v163", "v164", "v165", "v166", "v167", "v168", "v169", "v170", "v171", "v172", "v173", "v174", "v175", "v176", "v177", "v178", "v179", "v180", "v181", "v182", "v183", "v184", "v185", "v186", "v187", "v188", "v189", "v190", "v191", "v192", "v193", "v194", "v195", "v196", "v197", "v198", "v199", "v200", "v201", "v202", "v203", "v204", "v205", "v206", "v207", "v208", "v209", "v210", "v211", "v212", "v213", "v214", "v215", "v216", "v217", "v218", "v219", "v220", "v221", "v222", "v223", "v224", "v225", "v226", "v227", "v228", "v229", "v230", "v231", "v232", "v233", "v234", "v235", "v236", "v237", "v238", "v239", "v240", "v241", "v242", "v243", "v244", "v245", "v246", "v247", "v248", "v249", "v250", "v251", "v252", "v253", "v254", "v255", "a0", "a1", "a2", "a3", "a4", "a5", "a6", "a7", "a8", "a9", "a10", "a11", "a12", "a13", "a14", "a15", "a16", "a17", "a18", "a19", "a20", "a21", "a22", "a23", "a24", "a25", "a26", "a27", "a28", "a29", "a30", "a31", "a32", "a33", "a34", "a35", "a36", "a37", "a38", "a39", "a40", "a41", "a42", "a43", "a44", "a45", "a46", "a47", "a48", "a49", "a50", "a51", "a52", "a53", "a54", "a55", "a56", "a57", "a58", "a59", "a60", "a61", "a62", "a63", "a64", "a65", "a66", "a67", "a68", "a69", "a70", "a71", "a72", "a73", "a74", "a75", "a76", "a77", "a78", "a79", "a80", "a81", "a82", "a83", "a84", "a85", "a86", "a87", "a88", "a89", "a90", "a91", "a92", "a93", "a94", "a95", "a96", "a97", "a98", "a99", "a100", "a101", "a102", "a103", "a104", "a105", "a106", "a107", "a108", "a109", "a110", "a111", "a112", "a113", "a114", "a115", "a116", "a117", "a118", "a119", "a120", "a121", "a122", "a123", "a124", "a125", "a126", "a127", "a128", "a129", "a130", "a131", "a132", "a133", "a134", "a135", "a136", "a137", "a138", "a139", "a140", "a141", "a142", "a143", "a144", "a145", "a146", "a147", "a148", "a149", "a150", "a151", "a152", "a153", "a154", "a155", "a156", "a157", "a158", "a159", "a160", "a161", "a162", "a163", "a164", "a165", "a166", "a167", "a168", "a169", "a170", "a171", "a172", "a173", "a174", "a175", "a176", "a177", "a178", "a179", "a180", "a181", "a182", "a183", "a184", "a185", "a186", "a187", "a188", "a189", "a190", "a191", "a192", "a193", "a194", "a195", "a196", "a197", "a198", "a199", "a200", "a201", "a202", "a203", "a204", "a205", "a206", "a207", "a208", "a209", "a210", "a211", "a212", "a213", "a214", "a215", "a216", "a217", "a218", "a219", "a220", "a221", "a222", "a223", "a224", "a225", "a226", "a227", "a228", "a229", "a230", "a231", "a232", "a233", "a234", "a235", "a236", "a237", "a238", "a239", "a240", "a241", "a242", "a243", "a244", "a245", "a246", "a247", "a248", "a249", "a250", "a251", "a252", "a253", "a254", "a255", "s20", "s21", "s22", "s23", "s24", "s25", "s26", "s27", "s28", "s29", "s30", "s31", "s36", "s37", "s38", "s39", "s40", "s41", "s42", "s43", "s44", "s45", "s46", "s47", "s48", "s49", "s50", "s51", "s52", "s53", "s54", "s55", "s56", "s57", "s58", "s59", "s60", "s61", "s62", "s63", "s64", "s65", "s66", "s67", "s68", "s69", "s70", "s71", "s72", "s73", "s74", "s75", "s76", "s77", "s78", "s79", "s80", "s81", "s82", "s83", "s84", "s85", "s86", "s87", "s88", "s89", "s90", "s91", "s92", "vcc", "scc", "memory"

#define RFL(x) __builtin_amdgcn_readfirstlane(x)
__device__ __forceinline__ void role_A(int a, int w, uint64_t ka) {
  const int h = a / 14, j = a % 14;
  const unsigned kvoff = (unsigned)(h * SK + (j * 4 + w) * 18 * 8) * 512u;
#define ROTF 0
  const unsigned rot = RFL((unsigned)(ROTF) % 18u);
  const unsigned odd = (unsigned)j & 1u;
  const unsigned has19 = RFL((1u - odd) * (((unsigned)w - 2u) >> 31) + odd * (((unsigned)(j - 4)) >> 31) * (((unsigned)w - 1u) >> 31));
  const unsigned xoff = RFL(has19 * ((unsigned)(h * SK + 8064 + (1 - odd) * ((j & ~1) + w) + odd * (14 + (j >> 1))) * 512u));
  const unsigned sq = XQ + h * 1024;
  const unsigned sp = XP + ((h * 4 + w) * 14 + j) * 272;
  const unsigned cqw = (unsigned)(32 * j + 8 * w) * 8192u + 512u * h;
  const unsigned cqx = (unsigned)(32 * j + 8 * w) * 2u;
  const unsigned cqo = XC + (unsigned)(h * 14 + j) * 4096u;
#define GRPSH 3
  const unsigned grp = ((unsigned)a >> GRPSH) & 1u;
  asm volatile(ATT_ASM
               :
               : [ka] "s"(ka), [grp] "s"(grp), [kvoff] "s"(kvoff), [xoff] "s"(xoff), [has19] "s"(has19),
                 [sq] "s"(sq), [sp] "s"(sp), [w] "s"(w), [cqw] "s"(cqw), [cqx] "s"(cqx), [cqo] "s"(cqo), [rot] "s"(rot),
                 [xend] "i"(XEND2)
               : ATT_CLOB);
}
__device__ __forceinline__ void role_S(int bid, int w, uint64_t ka) {
  asm volatile(SVC_ASM
               :
               : [ka] "s"(ka), [bid] "s"(bid), [w] "s"(w),
                 [xend] "i"(XEND2)
               : SVC_CLOB);
}
__global__ void __launch_bounds__(256, 1) mega(const u16* __restrict__ x, const u16* __restrict__ kc, const u16* __restrict__ vc,
                                               const u16* __restrict__ wq, const u16* __restrict__ wo, const u16* __restrict__ wg,
                                               const u16* __restrict__ wu, const u16* __restrict__ wd, void* ws, u16* out) {
  __shared__ __attribute__((aligned(16))) float lds[40960];
  const int bid = blockIdx.x;
  const int w = RFL(threadIdx.x >> 6);
  const uint64_t ka = (uint64_t)__builtin_amdgcn_kernarg_segment_ptr();
  asm volatile("" ::"s"((unsigned)(uintptr_t)(__attribute__((address_space(3))) float*)lds));
  if (bid >= ATT0) role_A(bid - ATT0, w, ka);
  else role_S(bid, w, ka);
}
extern "C" void kernel_launch(void* const* d_in, const int* in_sizes, int n_in, void* d_out, int out_size, void* d_ws, size_t ws_size,
                              hipStream_t stream) {
  mega<<<NBLK, 256, 0, stream>>>((const u16*)d_in[0], (const u16*)d_in[1], (const u16*)d_in[2], (const u16*)d_in[3], (const u16*)d_in[4],
                                 (const u16*)d_in[6], (const u16*)d_in[7], (const u16*)d_in[8], d_ws, (u16*)d_out);
}
```
